```python
import torch

def _softmax(x, axis=-1):
    return torch.softmax(x, dim=axis)
def _gelu(x):
    return torch.nn.functional.gelu(x, approximate="tanh")
def _silu(x):
    return torch.nn.functional.silu(x)
def _sigmoid(x):
    return torch.sigmoid(x)
def _layernorm(x, axis=-1, eps=1e-5):
    m = x.mean(dim=axis, keepdim=True)
    v = x.var(dim=axis, keepdim=True, unbiased=False)
    return (x - m) / torch.sqrt(v + eps)
def _scatter_add(tgt, idx, src, axis=0):
    return tgt.clone().index_add_(axis, idx, src)
def _segment_sum(src, seg, n):
    out = torch.zeros((n,) + src.shape[1:], dtype=src.dtype)
    return out.index_add_(0, seg, src)
def _pad(x, pw):
    flat = tuple(v for pair in reversed(pw) for v in pair)
    return torch.nn.functional.pad(x, flat)

kernel_name = 'standalone__gqa_block_cached__near_ridge'

def setup_inputs():

    _w = (lambda a: (lambda t: t.to(torch.bfloat16) if t.is_floating_point() else t)(torch.as_tensor(a)))
    B, Sq, D = 8, 96, 512
    Hq, Hkv, Sk, Dh = 32, 32, 512, 128
    assert Hq % Hkv == 0, (Hq, Hkv)
    return {
        "x": _w((lambda *s: torch.randn(*s, dtype=torch.bfloat16) * 0.8)(B, Sq, D)),
        "k_cache": _w((lambda *s: torch.randn(*s, dtype=torch.bfloat16) * 0.8)(B, Hkv, Sk, Dh)),
        "v_cache": _w((lambda *s: torch.randn(*s, dtype=torch.bfloat16) * 0.05)(B, Hkv, Sk, Dh)),
        "wq": _w((lambda *s: torch.randn(*s, dtype=torch.bfloat16) * 0.8)(D, Hq * Dh) * (D ** -0.5)),
        "wo": _w((lambda *s: torch.randn(*s, dtype=torch.bfloat16) * 0.05)(Hq * Dh, D) * ((Hq * Dh) ** -0.5)),
    }

def reference(x, k_cache, v_cache, wq, wo):
    B, Sq, D = x.shape
    Hq, Dh = 32, 128
    Hkv = k_cache.shape[1]
    rep = Hq // Hkv
    q = (lambda x, *d: x.permute(*d))((x @ wq).reshape(B, Sq, Hq, Dh), 0, 2, 1, 3)
    k = (lambda x, n, axis: torch.repeat_interleave(x, n, dim=axis))(k_cache, rep, axis=1)
    v = (lambda x, n, axis: torch.repeat_interleave(x, n, dim=axis))(v_cache, rep, axis=1)
    scores = torch.einsum("bhqd,bhkd->bhqk", q, k) * (Dh ** -0.5)
    p = _softmax(scores, axis=-1)
    o = torch.einsum("bhqk,bhkd->bhqd", p, v)
    return (lambda x, *d: x.permute(*d))(o, 0, 2, 1, 3).reshape(B, Sq, Hq * Dh) @ wo
```

```cpp
#include <hip/hip_runtime.h>
#include <hip/hip_bf16.h>

typedef unsigned v4u __attribute__((ext_vector_type(4)));
typedef unsigned v2u __attribute__((ext_vector_type(2)));
typedef float f32x4 __attribute__((ext_vector_type(4)));
typedef float f32x2 __attribute__((ext_vector_type(2)));
typedef __bf16 bf16x8 __attribute__((ext_vector_type(8)));
typedef __bf16 bf16x2 __attribute__((ext_vector_type(2)));
typedef short s16x4 __attribute__((ext_vector_type(4)));
#define LAS __attribute__((address_space(3)))
#define DI __device__ __forceinline__
#define KV_AUX 2

DI f32x4 mfma16(v4u a, v4u b, f32x4 c) {
    return __builtin_amdgcn_mfma_f32_16x16x32_bf16(__builtin_bit_cast(bf16x8, a), __builtin_bit_cast(bf16x8, b), c, 0, 0, 0);
}
DI unsigned cvtpk(float lo, float hi) { f32x2 v = {lo, hi}; bf16x2 b = __builtin_convertvector(v, bf16x2); return __builtin_bit_cast(unsigned, b); }
DI void transpose8x8(const v4u (&in)[8], v4u (&out)[8]) {
#pragma unroll
    for (int p = 0; p < 4; ++p)
#pragma unroll
        for (int q = 0; q < 4; ++q) {
            out[2 * p][q] = __builtin_amdgcn_perm(in[2 * q + 1][p], in[2 * q][p], 0x05040100u);
            out[2 * p + 1][q] = __builtin_amdgcn_perm(in[2 * q + 1][p], in[2 * q][p], 0x07060302u);
        }
}
DI __amdgpu_buffer_rsrc_t mkrsrc(const void* p, int bytes) { return __builtin_amdgcn_make_buffer_rsrc((void*)p, 0, bytes, 0x00020000); }

constexpr int REG_BYTES = 49152;
constexpr int ML_OFF = 3 * REG_BYTES;
constexpr int LDS1 = ML_OFF + 3072;

DI void reduce_scatter(f32x4 (&acc)[8][6], LAS char* L, int w, int lane) {
#pragma unroll
    for (int tp = 2; tp < 6; ++tp) {
        int owner = (tp >> 1) + w; owner = owner >= 3 ? owner - 3 : owner;
        const int src = (w - owner - 1) & 3;
        LAS char* base = L + owner * REG_BYTES + (src * 2 + (tp & 1)) * 8192 + lane * 16;
#pragma unroll
        for (int i = 0; i < 8; ++i) *(LAS f32x4*)(base + i * 1024) = acc[i][tp];
    }
    if (w == 3) {
#pragma unroll
        for (int tp = 0; tp < 2; ++tp) {
            LAS char* base = L + 0 * REG_BYTES + (2 * 2 + tp) * 8192 + lane * 16;
#pragma unroll
            for (int i = 0; i < 8; ++i) *(LAS f32x4*)(base + i * 1024) = acc[i][tp];
        }
    }
    __syncthreads();
    if (w < 3) {
#pragma unroll
        for (int src = 0; src < 3; ++src)
#pragma unroll
            for (int tp = 0; tp < 2; ++tp)
#pragma unroll
                for (int i = 0; i < 8; ++i) acc[i][tp] += *(LAS f32x4*)(L + w * REG_BYTES + (src * 2 + tp) * 8192 + i * 1024 + lane * 16);
    }
}

__global__ __launch_bounds__(256, 1) void attn_kernel(const void* __restrict__ x, const void* __restrict__ K, const void* __restrict__ V,
                                                      const void* __restrict__ wq, v4u* __restrict__ Ows) {
    extern __shared__ __attribute__((aligned(16))) char smem[];
    LAS char* L = (LAS char*)smem;
    const int tid = threadIdx.x, lane = tid & 63, w = __builtin_amdgcn_readfirstlane(tid >> 6);
    const int c16 = lane & 15, g = lane >> 4;
    const int xcd = blockIdx.x & 7, idx = blockIdx.x >> 3;
    const int h = xcd * 4 + (idx & 3), b = idx >> 2;
    const int bh = b * 32 + h;
    const __amdgpu_buffer_rsrc_t rx = mkrsrc((const char*)x + (size_t)b * 96 * 1024, 96 * 1024);
    const __amdgpu_buffer_rsrc_t rwq = mkrsrc((const char*)wq, 512 * 8192);
    const __amdgpu_buffer_rsrc_t rk = mkrsrc((const char*)K + (size_t)bh * 131072, 131072);
    const __amdgpu_buffer_rsrc_t rv = mkrsrc((const char*)V + (size_t)bh * 131072, 131072);

    f32x4 acc[8][6];
#pragma unroll
    for (int i = 0; i < 8; ++i)
#pragma unroll
        for (int t = 0; t < 6; ++t) acc[i][t] = f32x4{0.f, 0.f, 0.f, 0.f};
    v4u kr[8][4];
    {
        v4u wr[4][8], xr[4][6];
        const int wqoff = (128 * w + 8 * g) * 8192 + h * 256 + c16 * 16;
        const int xoff = c16 * 1024 + (128 * w + 8 * g) * 2;
        int trot[6];
#pragma unroll
        for (int t = 0; t < 6; ++t) trot[t] = ((t + 2 * w) % 6) * 16384;
#pragma unroll
        for (int s = 0; s < 3; ++s) {
#pragma unroll
            for (int j = 0; j < 8; ++j) wr[s][j] = __builtin_amdgcn_raw_buffer_load_b128(rwq, wqoff + (32 * s + j) * 8192, 0, 0);
#pragma unroll
            for (int t = 0; t < 6; ++t) xr[s][t] = __builtin_amdgcn_raw_buffer_load_b128(rx, xoff + s * 64, trot[t], 0);
        }
        __builtin_amdgcn_sched_barrier(0);
        const int koff = (128 * w + c16) * 256 + g * 16;
#pragma unroll
        for (int s = 0; s < 4; ++s) {
            v4u a[8];
            transpose8x8(wr[s], a);
#pragma unroll
            for (int i = 0; i < 8; ++i)
#pragma unroll
                for (int t = 0; t < 6; ++t) acc[i][t] = mfma16(a[i], xr[s][t], acc[i][t]);
            __builtin_amdgcn_sched_barrier(0);
            if (s == 0) {
#pragma unroll
                for (int j = 0; j < 8; ++j) wr[3][j] = __builtin_amdgcn_raw_buffer_load_b128(rwq, wqoff + (32 * 3 + j) * 8192, 0, 0);
#pragma unroll
                for (int t = 0; t < 6; ++t) xr[3][t] = __builtin_amdgcn_raw_buffer_load_b128(rx, xoff + 3 * 64, trot[t], 0);
            } else {
                const int k0 = s == 1 ? 0 : (s == 2 ? 3 : 6), k1 = s == 1 ? 3 : (s == 2 ? 6 : 8);
#pragma unroll
                for (int kk = k0; kk < k1; ++kk)
#pragma unroll
                    for (int ss = 0; ss < 4; ++ss) kr[kk][ss] = __builtin_amdgcn_raw_buffer_load_b128(rk, koff + kk * 4096 + ss * 64, 0, KV_AUX);
            }
            __builtin_amdgcn_sched_barrier(0);
        }
    }
    v4u vr[4][8];
    const int voff = (128 * w + 4 * g) * 256 + c16 * 16;
#pragma unroll
    for (int u = 0; u < 1; ++u)
#pragma unroll
        for (int j = 0; j < 8; ++j) vr[u][j] = __builtin_amdgcn_raw_buffer_load_b128(rv, voff + (32 * u + 16 * (j >> 2) + (j & 3)) * 256, 0, KV_AUX);
    __builtin_amdgcn_sched_barrier(0);
    reduce_scatter(acc, L, w, lane);
    if (w < 3) {
#pragma unroll
        for (int tp = 0; tp < 2; ++tp)
#pragma unroll
            for (int r = 0; r < 4; ++r) {
                v4u q4 = {cvtpk(acc[0][tp][r], acc[1][tp][r]), cvtpk(acc[2][tp][r], acc[3][tp][r]), cvtpk(acc[4][tp][r], acc[5][tp][r]), cvtpk(acc[6][tp][r], acc[7][tp][r])};
                *(LAS v4u*)(L + w * REG_BYTES + tp * 4096 + g * 1024 + (c16 + 16 * r) * 16) = q4;
            }
    }
    __syncthreads();
    v4u pf[4][6];
    constexpr float CS = 0.08838834764831845f * 1.4426950408889634f;
#pragma unroll
    for (int t = 0; t < 6; ++t) {
        const int ta = (t + 2 * w) % 6;
        LAS char* qb = L + (ta >> 1) * REG_BYTES + (ta & 1) * 4096 + lane * 16;
        v4u qf[4];
#pragma unroll
        for (int s = 0; s < 4; ++s) qf[s] = *(LAS v4u*)(qb + s * 1024);
        f32x4 sa[8];
#pragma unroll
        for (int kt = 0; kt < 8; ++kt) {
            sa[kt] = f32x4{0.f, 0.f, 0.f, 0.f};
#pragma unroll
            for (int s = 0; s < 4; ++s) sa[kt] = mfma16(kr[kt][s], qf[s], sa[kt]);
        }
        float m = sa[0][0];
#pragma unroll
        for (int kt = 0; kt < 8; ++kt)
#pragma unroll
            for (int r = 0; r < 4; ++r) m = fmaxf(m, sa[kt][r]);
        m = fmaxf(m, __shfl_xor(m, 16));
        m = fmaxf(m, __shfl_xor(m, 32));
        const float mc = -m * CS;
        float l = 0.f;
#pragma unroll
        for (int kt = 0; kt < 8; ++kt)
#pragma unroll
            for (int r = 0; r < 4; ++r) { float p = __builtin_amdgcn_exp2f(fmaf(sa[kt][r], CS, mc)); sa[kt][r] = p; l += p; }
        l += __shfl_xor(l, 16);
        l += __shfl_xor(l, 32);
#pragma unroll
        for (int u2 = 0; u2 < 4; ++u2)
            pf[u2][t] = v4u{cvtpk(sa[2 * u2][0], sa[2 * u2][1]), cvtpk(sa[2 * u2][2], sa[2 * u2][3]), cvtpk(sa[2 * u2 + 1][0], sa[2 * u2 + 1][1]), cvtpk(sa[2 * u2 + 1][2], sa[2 * u2 + 1][3])};
        *(LAS f32x2*)(L + ML_OFF + ((w * 6 + ta) * 16 + c16) * 8) = f32x2{m, l};
    }
    __builtin_amdgcn_sched_barrier(0);
#pragma unroll
    for (int u = 1; u < 4; ++u)
#pragma unroll
        for (int j = 0; j < 8; ++j) vr[u][j] = __builtin_amdgcn_raw_buffer_load_b128(rv, voff + (32 * u + 16 * (j >> 2) + (j & 3)) * 256, 0, KV_AUX);
    __builtin_amdgcn_sched_barrier(0);
#pragma unroll
    for (int i = 0; i < 8; ++i)
#pragma unroll
        for (int t = 0; t < 6; ++t) acc[i][t] = f32x4{0.f, 0.f, 0.f, 0.f};
#pragma unroll
    for (int u = 0; u < 4; ++u) {
        v4u a[8];
        transpose8x8(vr[u], a);
#pragma unroll
        for (int i = 0; i < 8; ++i)
#pragma unroll
            for (int t = 0; t < 6; ++t) acc[i][t] = mfma16(a[i], pf[u][t], acc[i][t]);
    }
    __syncthreads();
#pragma unroll
    for (int t = 0; t < 6; ++t) {
        f32x2 ml[4];
        const int ta = (t + 2 * w) % 6;
#pragma unroll
        for (int ww = 0; ww < 4; ++ww) ml[ww] = *(LAS f32x2*)(L + ML_OFF + ((ww * 6 + ta) * 16 + c16) * 8);
        const float M = fmaxf(fmaxf(ml[0][0], ml[1][0]), fmaxf(ml[2][0], ml[3][0]));
        float Ls = 0.f;
#pragma unroll
        for (int ww = 0; ww < 4; ++ww) Ls += __builtin_amdgcn_exp2f((ml[ww][0] - M) * CS) * ml[ww][1];
        const float mw = *(LAS float*)(L + ML_OFF + ((w * 6 + ta) * 16 + c16) * 8);
        const float f = __builtin_amdgcn_exp2f((mw - M) * CS) / Ls;
#pragma unroll
        for (int i = 0; i < 8; ++i) acc[i][t] *= f;
    }
    reduce_scatter(acc, L, w, lane);
    if (w < 3) {
#pragma unroll
        for (int tp = 0; tp < 2; ++tp)
#pragma unroll
            for (int r = 0; r < 4; ++r) {
                v4u o4 = {cvtpk(acc[0][tp][r], acc[1][tp][r]), cvtpk(acc[2][tp][r], acc[3][tp][r]), cvtpk(acc[4][tp][r], acc[5][tp][r]), cvtpk(acc[6][tp][r], acc[7][tp][r])};
                Ows[(((size_t)bh * 4 + r) * 6 + 2 * w + tp) * 64 + lane] = o4;
            }
    }
}

__global__ __launch_bounds__(256, 1) void oproj_kernel(const v4u* __restrict__ Ows, const void* __restrict__ wo, void* __restrict__ out) {
    extern __shared__ __attribute__((aligned(16))) char smem[];
    LAS char* L = (LAS char*)smem;
    const int tid = threadIdx.x, lane = tid & 63, w = __builtin_amdgcn_readfirstlane(tid >> 6);
    const int c16 = lane & 15, g = lane >> 4;
    const int xcd = blockIdx.x & 7, idx = blockIdx.x >> 3;
    const int b = (xcd >> 1) * 2 + ((idx >> 1) & 1), mh = idx & 1, nj = (xcd & 1) * 8 + (idx >> 2);
    const __amdgpu_buffer_rsrc_t rwo = mkrsrc(wo, 4096 * 1024);
    f32x4 acc[2][3];
#pragma unroll
    for (int nt = 0; nt < 2; ++nt)
#pragma unroll
        for (int t = 0; t < 3; ++t) acc[nt][t] = f32x4{0.f, 0.f, 0.f, 0.f};
    LAS char* st = L + w * 16384;
    const int wrow = lane >> 2, wch = lane & 3;
#pragma unroll 2
    for (int hh = 0; hh < 8; ++hh) {
        const int h = 8 * w + hh;
        v4u wr[8];
#pragma unroll
        for (int i = 0; i < 8; ++i) wr[i] = __builtin_amdgcn_raw_buffer_load_b128(rwo, (h * 128 + 16 * i + wrow) * 1024 + nj * 64 + wch * 16, 0, 0);
        v4u of[4][3];
#pragma unroll
        for (int r = 0; r < 4; ++r)
#pragma unroll
            for (int t = 0; t < 3; ++t) of[r][t] = Ows[(((size_t)(b * 32 + h) * 4 + r) * 6 + 3 * mh + t) * 64 + lane];
        LAS char* sb = st + (hh & 1) * 8192;
#pragma unroll
        for (int i = 0; i < 8; ++i) { const int row = 16 * i + wrow; *(LAS v4u*)(sb + row * 64 + ((wch ^ (((row >> 5) & 1) << 1)) * 16)) = wr[i]; }
#pragma unroll
        for (int r = 0; r < 4; ++r)
#pragma unroll
            for (int nt = 0; nt < 2; ++nt) {
                const int q4 = (lane & 15) >> 2, p = lane & 3;
                const int row0 = 32 * g + 8 * r + q4;
                const int cb = ((nt ^ (g & 1)) * 32) + p * 8;
                s16x4 lo = __builtin_amdgcn_ds_read_tr16_b64_v4i16((LAS s16x4*)(sb + row0 * 64 + cb));
                s16x4 hi = __builtin_amdgcn_ds_read_tr16_b64_v4i16((LAS s16x4*)(sb + (row0 + 4) * 64 + cb));
                v2u lo2 = __builtin_bit_cast(v2u, lo), hi2 = __builtin_bit_cast(v2u, hi);
                v4u a = {lo2[0], lo2[1], hi2[0], hi2[1]};
#pragma unroll
                for (int t = 0; t < 3; ++t) acc[nt][t] = mfma16(a, of[r][t], acc[nt][t]);
            }
    }
    __syncthreads();
#pragma unroll
    for (int nt = 0; nt < 2; ++nt)
#pragma unroll
        for (int t = 0; t < 3; ++t) *(LAS f32x4*)(L + ((w * 6 + nt * 3 + t) * 64 + lane) * 16) = acc[nt][t];
    __syncthreads();
    for (int f = w; f < 6; f += 4) {
        f32x4 s = {0.f, 0.f, 0.f, 0.f};
#pragma unroll
        for (int ww = 0; ww < 4; ++ww) s += *(LAS f32x4*)(L + ((ww * 6 + f) * 64 + lane) * 16);
        const int nt = f / 3, t = f % 3;
        v2u o = {cvtpk(s[0], s[1]), cvtpk(s[2], s[3])};
        *(v2u*)((char*)out + ((size_t)(b * 96 + 48 * mh + 16 * t + c16) * 512 + 32 * nj + 16 * nt + 4 * g) * 2) = o;
    }
}

extern "C" void kernel_launch(void* const* d_in, const int* in_sizes, int n_in, void* d_out, int out_size, void* d_ws, size_t ws_size, hipStream_t stream) {
    (void)hipFuncSetAttribute((const void*)attn_kernel, hipFuncAttributeMaxDynamicSharedMemorySize, LDS1);
    (void)hipFuncSetAttribute((const void*)oproj_kernel, hipFuncAttributeMaxDynamicSharedMemorySize, 65536);
    attn_kernel<<<256, 256, LDS1, stream>>>(d_in[0], d_in[1], d_in[2], d_in[3], (v4u*)d_ws);
    oproj_kernel<<<256, 256, 65536, stream>>>((const v4u*)d_ws, d_in[4], d_out);
}
```

```cpp
#include <hip/hip_runtime.h>
#include <hip/hip_bf16.h>
#include <type_traits>

typedef unsigned v4u __attribute__((ext_vector_type(4)));
typedef unsigned v2u __attribute__((ext_vector_type(2)));
typedef float f32x16 __attribute__((ext_vector_type(16)));
typedef float f32x2 __attribute__((ext_vector_type(2)));
typedef __bf16 bf16x8 __attribute__((ext_vector_type(8)));
typedef __bf16 bf16x2 __attribute__((ext_vector_type(2)));
typedef short s16x4 __attribute__((ext_vector_type(4)));
typedef unsigned long long u64;
#define LAS __attribute__((address_space(3)))
#define GAS __attribute__((address_space(1)))
#define DI __device__ __forceinline__
#define RLX_AGENT __ATOMIC_RELAXED, __HIP_MEMORY_SCOPE_AGENT

#define KV_AUX 2
#define KV_D 4
#define W_D 3
#define CLR_SLEEP 4
#define LD_SLEEP 1
#define CP_SLEEP 1
#define KV_PRE 0
#define X_D 3
#define LD_PRIO 3
#define WO_D 2
#define KV_START 2

#define STAMP(i)
#define STAMP_DECL
#define STAMP_FLUSH(ws)

constexpr int SLOT = 8192, R1 = 8, NWO1 = 11;
constexpr int WQ_BASE = 0, X_BASE = 8 * SLOT, KV_BASE = 11 * SLOT, CTL = KV_BASE + R1 * SLOT;
constexpr int LDS_BYTES = CTL + 256;
constexpr int C_FULLK = 0, C_FULLW = 4, C_PK = 16  , C_PW = 32  , C_PX = 48  , C_READY = 64, C_DONE = 68;
constexpr size_t WS_EPOCH = 0;
constexpr size_t WS_F1 = 4096;
constexpr size_t WS_F2 = WS_F1 + 768 * 128;
constexpr size_t WS_O = 524288;
constexpr size_t WS_P = WS_O + 256 * 24576;
constexpr int WS_BYTES = (int)(WS_P + 256 * 24576);
constexpr unsigned SPIN_CAP = 1u << 22;

constexpr int xseq(int cbi) { return cbi == 0 ? 0 : KV_PRE + 3 * cbi; }
constexpr int kseq(int c) { return c < KV_PRE ? 3 + c + 1 : 12 + c + 1; }
DI f32x16 mfma32(v4u a, v4u b, f32x16 c) {
    return __builtin_amdgcn_mfma_f32_32x32x16_bf16(__builtin_bit_cast(bf16x8, a), __builtin_bit_cast(bf16x8, b), c, 0, 0, 0);
}
DI unsigned cvtpk(float lo, float hi) { f32x2 v = {lo, hi}; bf16x2 b = __builtin_convertvector(v, bf16x2); return __builtin_bit_cast(unsigned, b); }
DI __amdgpu_buffer_rsrc_t mkrsrc(const void* p, int bytes) { return __builtin_amdgcn_make_buffer_rsrc((void*)p, 0, bytes, 0x00020000); }
DI unsigned rfl(unsigned x) { return __builtin_amdgcn_readfirstlane(x); }
DI v4u pack8(const f32x16& a, int s) {
    return v4u{cvtpk(a[8 * s], a[8 * s + 1]), cvtpk(a[8 * s + 2], a[8 * s + 3]), cvtpk(a[8 * s + 4], a[8 * s + 5]), cvtpk(a[8 * s + 6], a[8 * s + 7])};
}
DI float swapmax(float x) { auto r = __builtin_amdgcn_permlane32_swap(__float_as_uint(x), __float_as_uint(x), false, false); return fmaxf(__uint_as_float(r[0]), __uint_as_float(r[1])); }
DI float swapsum(float x) { auto r = __builtin_amdgcn_permlane32_swap(__float_as_uint(x), __float_as_uint(x), false, false); return __uint_as_float(r[0]) + __uint_as_float(r[1]); }
DI float max3f(float a, float b, float c) { float r; asm("v_max3_f32 %0, %1, %2, %3" : "=v"(r) : "v"(a), "v"(b), "v"(c)); return r; }
DI float fadd_s(float a, float b) { float r; asm("v_add_f32_e32 %0, %1, %2" : "=v"(r) : "v"(a), "v"(b)); return r; }
#define SB() __builtin_amdgcn_sched_barrier(0)
#define OPAQUE(x) asm volatile("" : "+v"(x))
#define CBAR() asm volatile("" ::: "memory")
#define VMCNT(n) asm volatile("s_waitcnt vmcnt(%0)" :: "i"(n) : "memory")
#define LGKM0() asm volatile("s_waitcnt lgkmcnt(0)" ::: "memory")

DI unsigned lds_ld(unsigned addr) { unsigned r; asm volatile("ds_read_b32 %0, %1\n\ts_waitcnt lgkmcnt(0)" : "=v"(r) : "v"(addr) : "memory"); return r; }
DI void lds_st(unsigned addr, unsigned v) { asm volatile("ds_write_b32 %0, %1" :: "v"(addr), "v"(v) : "memory"); }
DI unsigned lds_min3(unsigned addr) {
    unsigned a, b, c;
    asm volatile("ds_read_b32 %0, %3\n\tds_read_b32 %1, %3 offset:4\n\tds_read_b32 %2, %3 offset:8\n\ts_waitcnt lgkmcnt(0)" : "=&v"(a), "=&v"(b), "=&v"(c) : "v"(addr) : "memory");
    return rfl(min(a, min(b, c)));
}

template <int AUX> DI void dma_chunk(__amdgpu_buffer_rsrc_t rs, LAS char* slot, const int (&voff)[4], int soff, int half) {
    __builtin_amdgcn_raw_ptr_buffer_load_lds(rs, (LAS void*)slot, 16, voff[0], soff, 0, AUX);
    __builtin_amdgcn_raw_ptr_buffer_load_lds(rs, (LAS void*)slot, 16, voff[1], soff, 1024, AUX);
    __builtin_amdgcn_raw_ptr_buffer_load_lds(rs, (LAS void*)slot, 16, voff[2], soff, 2048, AUX);
    __builtin_amdgcn_raw_ptr_buffer_load_lds(rs, (LAS void*)slot, 16, voff[3], soff, 3072, AUX);
    __builtin_amdgcn_raw_ptr_buffer_load_lds(rs, (LAS void*)(slot + 4096), 16, voff[0], soff + half, 0, AUX);
    __builtin_amdgcn_raw_ptr_buffer_load_lds(rs, (LAS void*)(slot + 4096), 16, voff[1], soff + half, 1024, AUX);
    __builtin_amdgcn_raw_ptr_buffer_load_lds(rs, (LAS void*)(slot + 4096), 16, voff[2], soff + half, 2048, AUX);
    __builtin_amdgcn_raw_ptr_buffer_load_lds(rs, (LAS void*)(slot + 4096), 16, voff[3], soff + half, 3072, AUX);
}
DI v2u tr_rd(unsigned addr, int off) { return __builtin_bit_cast(v2u, __builtin_amdgcn_ds_read_tr16_b64_v4i16((LAS s16x4*)(size_t)(addr + off))); }
DI v4u row_rd(unsigned addr) { return *(LAS v4u*)(size_t)addr; }
DI v4u tr_frag(unsigned tb, int off, int s, int ct) {
    v2u l2 = tr_rd(tb ^ (unsigned)(64 * ct), off + s * 4096), h2 = tr_rd(tb ^ (unsigned)(64 * ct + 16), off + s * 4096 + 1024);
    return v4u{l2[0], l2[1], h2[0], h2[1]};
}
DI void tr_frag_g(unsigned tb, int off, int g, v4u (&fr)[2][4]) { fr[g & 1][g >> 1] = tr_frag(tb, off, g & 1, g >> 1); }
DI void tr_chunk(unsigned tb, int off, v4u (&fr)[2][4]) {
#pragma unroll
    for (int g = 0; g < 8; ++g) tr_frag_g(tb, off, g, fr);
}
template <bool NEXT> DI void gemm_step(f32x16 (&acc)[4], const v4u (&cur)[2][4], v4u b0, v4u b1, unsigned tb, int off, v4u (&nxt)[2][4]) {
#pragma unroll
    for (int g = 0; g < 8; ++g) {
        acc[g >> 1] = mfma32(cur[g & 1][g >> 1], (g & 1) ? b1 : b0, acc[g >> 1]);
        if (NEXT) tr_frag_g(tb, off, g, nxt);
        SB();
    }
}

__global__ __launch_bounds__(320, 2) void fused_kernel(const void* __restrict__ x, const void* __restrict__ K, const void* __restrict__ V,
                                                      const void* __restrict__ wq, const void* __restrict__ wo, void* __restrict__ out, char* ws) {
    extern __shared__ __attribute__((aligned(16))) char smem[];
    LAS char* L = (LAS char*)smem;
    const unsigned Lb = (unsigned)(size_t)L;
    const int tid = threadIdx.x, lane = tid & 63, w = rfl(tid >> 6);
    const int xcd = blockIdx.x & 7, idx = blockIdx.x >> 3;
    const int hl = idx & 3, b = idx >> 2, h = xcd * 4 + hl, bh = b * 32 + h;
    const int wgid = bh;
    STAMP_DECL
    STAMP(0);
    const __amdgpu_buffer_rsrc_t rws = mkrsrc(ws, WS_BYTES);
    const int cb0 = ((b >> 1) + hl) & 3, rotp = b % 3;
    u64 tag = 0;

    if (w >= 3) {
        const int pos = lane & 15, rq = lane >> 4;
        int vo1k[4];
#pragma unroll
        for (int k = 0; k < 4; ++k) vo1k[k] = (4 * k + rq) * 1024 + 16 * (pos ^ (rq << 2) ^ k) - 1024 * k;
        const __amdgpu_buffer_rsrc_t rwo = mkrsrc(wo, 4096 * 1024);
        auto wo_off = [&](int ci) -> int {
            int head = hl;
            if (ci >= 4) { int ps = (ci >> 2) - 1 + rotp; ps = ps >= 3 ? ps - 3 : ps; head = (hl + 1 + ps) & 3; }
            return ((xcd * 4 + head) * 128 + (ci & 3) * 32) * 1024 + hl * 256;
        };
        __builtin_amdgcn_s_setprio(LD_PRIO);
        unsigned prog = 0;
        unsigned fulla = Lb + CTL + (w == 3 ? C_FULLK : C_FULLW);
        OPAQUE(fulla);
        auto gate = [&](int cw, unsigned need) {
            if (__builtin_expect(prog < need, 0)) {
                for (unsigned sp = 0; prog < need && sp < SPIN_CAP; ++sp) { if (sp) __builtin_amdgcn_s_sleep(LD_SLEEP); prog = lds_min3(Lb + CTL + cw); }
            }
        };
        if (w == 3) {
            const __amdgpu_buffer_rsrc_t rx = mkrsrc((const char*)x + (size_t)b * 98304, 98304);
            const __amdgpu_buffer_rsrc_t rk = mkrsrc((const char*)K + (size_t)bh * 131072, 131072);
            const __amdgpu_buffer_rsrc_t rv = mkrsrc((const char*)V + (size_t)bh * 131072, 131072);
            int vo256[4];
#pragma unroll
            for (int k = 0; k < 4; ++k) vo256[k] = rq * 256 + 16 * (pos ^ (rq << 2) ^ k);
            tag = __hip_atomic_load((GAS u64*)(ws + WS_EPOCH + b * 256), RLX_AGENT) + 1;
#pragma unroll
            for (int i = 0; i < 12; ++i) {
                const int cbi = i / 3, r = i % 3;
                if (i >= X_D) { VMCNT(8 * (X_D - 1)); lds_st(fulla, i - X_D + 1); }
                if (i == 3) STAMP(4); if (i == 6) STAMP(5); if (i == 9) STAMP(7);
                if (i == 6 || i == 9) gate(C_PX, cbi - 1);
                if (i == 6) STAMP(6); if (i == 9) STAMP(8);
                dma_chunk<0>(rx, L + ((cbi & 1) ? KV_BASE + 5 * SLOT : X_BASE) + r * SLOT, vo1k, r * 32768 + ((cbi + cb0) & 3) * 256, 16384);
                if (i == X_D - 1) __builtin_amdgcn_s_barrier();
            }
            unsigned pubd = 12 - X_D;
            prog = lds_min3(Lb + CTL + C_PW);
            if (prog < KV_START) {
#pragma unroll
                for (int d = X_D - 1; d >= 0; --d) { VMCNT(8 * d); lds_st(fulla, 12 - d); }
                pubd = 12;
                gate(C_PW, KV_START);
            }
            STAMP(1);
            prog = 0;
#pragma unroll 1
            for (int p = 0; p < 16; ++p) {
                const int c = 2 * p;
                LAS char* sl = L + KV_BASE + (c & (R1 - 1)) * SLOT;
                VMCNT(8 * (KV_D - 1)); lds_st(fulla, max(pubd, (unsigned)(12 + c - KV_D + 1)));
                if (c >= R1) gate(C_PK, c - R1 + 1);
                if (p > 0) dma_chunk<KV_AUX>(rv, sl, vo256, (p - 1) * 8192, 4096);
                else dma_chunk<KV_AUX>(rk, sl, vo256, 0, 4096);
                VMCNT(8 * (KV_D - 1)); lds_st(fulla, max(pubd, (unsigned)(12 + c + 1 - KV_D + 1)));
                if (c >= R1) gate(C_PK, c - R1 + 2);
                else if (p == 2) { gate(C_PX, 4); prog = 0; }
                if (p < 15) dma_chunk<KV_AUX>(rk, sl + SLOT, vo256, (p + 1) * 8192, 4096);
                else dma_chunk<KV_AUX>(rv, sl + SLOT, vo256, 15 * 8192, 4096);
            }
            STAMP(2);
#pragma unroll 1
            for (int c = 32; c < 32 + 16 - NWO1; ++c) {
                VMCNT(8 * (KV_D - 1)); lds_st(fulla, 12 + c - KV_D + 1);
                gate(C_PK, c - R1 + 1);
                dma_chunk<0>(rwo, L + KV_BASE + (c & (R1 - 1)) * SLOT, vo1k, wo_off(NWO1 + c - 32), 16384);
            }
#pragma unroll
            for (int d = KV_D - 1; d >= 0; --d) { VMCNT(8 * d); lds_st(fulla, 12 + 32 + 16 - NWO1 - d); }
            STAMP(3);
        } else {
            const __amdgpu_buffer_rsrc_t rwq = mkrsrc(wq, 512 * 8192);
            int vo8k[4];
#pragma unroll
            for (int k = 0; k < 4; ++k) vo8k[k] = (4 * k + rq) * 8192 + 16 * (pos ^ (rq << 2) ^ k) - 1024 * k;
#pragma unroll
            for (int n = 0; n < 16; ++n) {
                if (n >= W_D) { VMCNT(8 * (W_D - 1)); lds_st(fulla, n - W_D + 1); }
                if (n >= 8) gate(C_PW, n - 7);
                dma_chunk<0>(rwq, L + WQ_BASE + (n & 7) * SLOT, vo8k, ((((n >> 2) + cb0) & 3) * 128 + ((n & 3) ^ (2 * (b & 1))) * 32) * 8192 + h * 256, 131072);
                if (n == 1) __builtin_amdgcn_s_barrier();
            }
            STAMP(1);
#pragma unroll
            for (int d = W_D - 1; d >= 0; --d) { VMCNT(8 * d); lds_st(fulla, 16 - d); }
            gate(C_PW, 16);
#pragma unroll 1
            for (int ci = 0; ci < NWO1; ++ci) {
                dma_chunk<0>(rwo, L + WQ_BASE + ci * SLOT, vo1k, wo_off(ci), 16384);
                VMCNT(WO_D * 8 - 8); if (ci + 1 >= WO_D) lds_st(fulla, 17 + ci - WO_D + 1);
            }
            VMCNT(0); lds_st(fulla, 16 + NWO1);
            STAMP(2);
        }
    } else {
        const int t = w;
        if (tid < 64) *(LAS unsigned*)(L + CTL + tid * 4) = 0u;
        LGKM0();
        __builtin_amdgcn_s_barrier();
        tag = __hip_atomic_load((GAS u64*)(ws + WS_EPOCH + b * 256), RLX_AGENT) + 1;
        const int r32 = lane & 31, hh = lane >> 5;
        volatile LAS unsigned* ctl = (volatile LAS unsigned*)(L + CTL);
        const int fr_ = ((r32 & 3) << 2) | ((r32 >> 2) & 3);
        unsigned rbx = Lb + t * SLOT + ((256 * r32 + 16 * (hh ^ fr_)) ^ (128 * (b & 1)));
        const int pr = (r32 & 19) | ((r32 & 4) << 1) | ((r32 & 8) >> 1);
        const int fp_ = ((pr & 3) << 2) | ((pr >> 2) & 3);
        unsigned rbk = Lb + KV_BASE + 256 * pr + 16 * (hh ^ fp_);
        const int blk = (lane >> 4) & 1, q4 = (lane & 15) >> 2, p4 = lane & 3;
        unsigned tb0 = Lb + 256 * (8 * hh + q4) + 64 * q4 + 16 * ((2 * blk + (p4 & 1)) ^ (2 * hh)) + 8 * (p4 >> 1);
        unsigned availW = 0, availK = 0;
        auto waitW = [&](unsigned need) { if (__builtin_expect(availW < need, 0)) for (unsigned sp = 0; availW < need && sp < SPIN_CAP; ++sp) { if (sp) __builtin_amdgcn_s_sleep(CP_SLEEP); availW = rfl(ctl[C_FULLW / 4]); } CBAR(); };
        auto waitKs = [&](unsigned need) { if (__builtin_expect(availK < need, 0)) for (unsigned sp = 0; availK < need && sp < SPIN_CAP; ++sp) { if (sp) __builtin_amdgcn_s_sleep(CP_SLEEP); availK = rfl(ctl[C_FULLK / 4]); } CBAR(); };
        auto waitK = [&](unsigned n) { waitKs(12 + n); };
        f32x16 acc[4];
#pragma unroll
        for (int d = 0; d < 4; ++d) acc[d] = f32x16{};
        {
            v4u fa[2][4], fb[2][4], xf[32];
            waitW(1);
            tr_chunk(tb0, 0, fa);
            ctl[C_PW / 4 + t] = 1;
            waitKs(xseq(0) + t + 1);
#pragma unroll
            for (int ks = 0; ks < 8; ++ks) xf[ks] = row_rd(((rbx + X_BASE) ^ (unsigned)(32 * ks)));
            ctl[C_PX / 4 + t] = 1;
#pragma unroll
            for (int n = 0; n < 16; ++n) {
                const int xq = n == 1 ? 1 : (n == 4 ? 2 : (n == 7 ? 3 : 0));
                if (n < 15) waitW(n + 2);
                if (xq) waitKs(xseq(xq) + t + 1);
                const int offn = ((n + 1) & 7) * SLOT;
                const unsigned xn = rbx + ((xq & 1) ? KV_BASE + 5 * SLOT : X_BASE);
                v2u fw;
                SB();
#pragma unroll
                for (int g = 0; g < 8; ++g) {
                    if (n & 1) { acc[g >> 1] = mfma32(fb[g & 1][g >> 1], xf[2 * n + (g & 1)], acc[g >> 1]); if (n < 15) tr_frag_g(tb0, offn, g, fa); }
                    else { acc[g >> 1] = mfma32(fa[g & 1][g >> 1], xf[2 * n + (g & 1)], acc[g >> 1]); tr_frag_g(tb0, offn, g, fb); }
                    if (xq) xf[8 * xq + g] = row_rd(xn ^ (unsigned)(32 * g));
                    if (g == 3) fw = *(volatile LAS v2u*)(L + CTL);
                    if (g == 7) {
                        if (n < 15) ctl[C_PW / 4 + t] = n + 2;
                        if (xq) ctl[C_PX / 4 + t] = xq + 1;
                        availK = rfl(fw[0]); availW = rfl(fw[1]);
                    }
                    SB();
                }
            }
        }
        asm volatile("" :: "v"(tag));
        STAMP(1);
        constexpr float CS = 0.08838834764831845f * 1.4426950408889634f;
        constexpr float THR = 8.0f;
        v4u qf[8];
#pragma unroll
        for (int d = 0; d < 4; ++d) { acc[d] *= CS; qf[2 * d] = pack8(acc[d], 0); qf[2 * d + 1] = pack8(acc[d], 1); }
#pragma unroll
        for (int d = 0; d < 4; ++d) acc[d] = f32x16{};
        float lsum = 0.f;
        f32x16 sA, sB, negm; v4u kf[8], vf[2][4];
        unsigned tbk = tb0 + KV_BASE; OPAQUE(tbk);
        unsigned ctla = Lb + CTL + C_FULLK, pka = Lb + CTL + C_PK + 4 * t, relv = 2;
        OPAQUE(ctla); OPAQUE(pka); OPAQUE(relv);
        auto decide = [&](float tm, f32x16& nxt) {
            if (__builtin_expect(__builtin_amdgcn_ballot_w64(tm > THR) != 0ull, 0)) {
                const float dl = fmaxf(swapmax(tm), 0.f), f = __builtin_amdgcn_exp2f(-dl);
                lsum *= f;
#pragma unroll
                for (int d = 0; d < 4; ++d) acc[d] *= f;
#pragma unroll
                for (int i = 0; i < 16; ++i) { nxt[i] -= dl; negm[i] -= dl; }
            }
        };
        auto block = [&](f32x16& cur, f32x16& nxt, int vslot, auto KR) -> float {
            v4u p[2];
            float m = 0.f, m1 = 0.f, s0 = 0.f, s1 = 0.f; unsigned fk = 0;
            SB();
#pragma unroll
            for (int g = 0; g < 8; ++g) {
                nxt = mfma32(kf[g], qf[g], g == 0 ? negm : nxt);
                tr_frag_g(tbk, vslot * SLOT, g, vf);
                const int e0 = (3 * g + 1) / 2, e1 = (3 * (g + 1) + 1) / 2;
#pragma unroll
                for (int e = e0; e < e1; ++e) cur[e] = __builtin_amdgcn_exp2f(cur[e]);
                if (g >= 1) {
                    const int pp0 = ((3 * (g - 1) + 1) / 2) / 2, pp1 = e0 / 2;
#pragma unroll
                    for (int q = pp0; q < pp1; ++q) p[q >> 2][q & 3] = cvtpk(cur[2 * q], cur[2 * q + 1]);
                }
                SB();
            }
#pragma unroll
            for (int g = 0; g < 8; ++g) {
                acc[g & 3] = mfma32(vf[g >> 2][g & 3], p[g >> 2], acc[g & 3]);
                if (KR && g < 4) { kf[2 * g] = row_rd((rbk ^ (unsigned)(64 * g)) + (vslot + 1) * SLOT); kf[2 * g + 1] = row_rd((rbk ^ (unsigned)(64 * g + 32)) + (vslot + 1) * SLOT); }
                if (g == 0) { cur[12] = __builtin_amdgcn_exp2f(cur[12]); cur[13] = __builtin_amdgcn_exp2f(cur[13]); p[1][1] = cvtpk(cur[10], cur[11]); }
                if (g == 1) { cur[14] = __builtin_amdgcn_exp2f(cur[14]); cur[15] = __builtin_amdgcn_exp2f(cur[15]); p[1][2] = cvtpk(cur[12], cur[13]); }
                if (g == 2) { p[1][3] = cvtpk(cur[14], cur[15]); m = max3f(nxt[0], nxt[1], nxt[2]); m1 = max3f(nxt[3], nxt[4], nxt[5]); }
                if (g == 3) { m = max3f(m, nxt[6], nxt[7]); m1 = max3f(m1, nxt[8], nxt[9]); }
                if (g == 4) { m = max3f(m, nxt[10], nxt[11]); m1 = max3f(m1, nxt[12], nxt[13]); relv += KR ? 2 : 1; *(volatile LAS unsigned*)(size_t)pka = relv; fk = *(volatile LAS unsigned*)(size_t)ctla; }
                if (g == 5) { m = max3f(m, nxt[14], nxt[15]); m = max3f(m, m1, m1); }
                if (g >= 4) {
#pragma unroll
                    for (int e = 4 * (g - 4); e < 4 * (g - 4) + 4; e += 2) { s0 = fadd_s(s0, cur[e]); s1 = fadd_s(s1, cur[e + 1]); }
                }
                SB();
            }
            lsum += s0 + s1;
            OPAQUE(lsum);
            availK = rfl(fk);
            return m;
        };
        waitK(1);
#pragma unroll
        for (int ks = 0; ks < 8; ++ks) kf[ks] = row_rd(rbk ^ (unsigned)(32 * ks));
#pragma unroll
        for (int ks = 0; ks < 8; ++ks) sA = mfma32(kf[ks], qf[ks], ks == 0 ? f32x16{} : sA);
        waitK(2);
#pragma unroll
        for (int ks = 0; ks < 8; ++ks) kf[ks] = row_rd((rbk ^ (unsigned)(32 * ks)) + SLOT);
        *(volatile LAS unsigned*)(size_t)pka = relv;
        {
            float m = fmaxf(sA[0], sA[1]);
#pragma unroll
            for (int i = 2; i < 16; i += 2) m = fmaxf(fmaxf(m, sA[i]), sA[i + 1]);
            m = swapmax(m);
#pragma unroll
            for (int i = 0; i < 16; ++i) { sA[i] -= m; negm[i] = -m; }
            OPAQUE(negm);
        }
        STAMP(2);
        {
            constexpr std::integral_constant<bool, true> T{};
            constexpr std::integral_constant<bool, false> F{};
#pragma unroll 1
            for (int j = 0; j < 12; j += 4) {
                waitK(2 * j + 4); decide(block(sA, sB, 2, T), sB);
                waitK(2 * j + 6); decide(block(sB, sA, 4, T), sA);
                waitK(2 * j + 8); decide(block(sA, sB, 6, T), sB);
                waitK(2 * j + 10); decide(block(sB, sA, 0, T), sA);
            }
            waitK(28); decide(block(sA, sB, 2, T), sB);
            waitK(30); decide(block(sB, sA, 4, T), sA);
            waitK(31);
            decide(block(sA, sB, 6, F), sB);
            STAMP(3);
        }
        v4u pf[3][8];
        int pso[3];
#pragma unroll
        for (int s = 0; s < 3; ++s) {
            int ps = s + rotp; ps = ps >= 3 ? ps - 3 : ps;
            pso[s] = (int)WS_O + (b * 32 + xcd * 4 + ((hl + 1 + ps) & 3)) * 24576 + t * 8192;
        }
        v4u p15[2];
        {
            float s0 = 0.f, s1 = 0.f;
#pragma unroll
            for (int g = 0; g < 8; ++g) {
                const float e0 = __builtin_amdgcn_exp2f(sB[2 * g]), e1 = __builtin_amdgcn_exp2f(sB[2 * g + 1]);
                s0 += e0; s1 += e1; p15[g >> 2][g & 3] = cvtpk(e0, e1);
            }
            lsum += s0 + s1;
        }
        float inv = 1.0f / swapsum(lsum);
        OPAQUE(inv); OPAQUE(p15[0]); OPAQUE(p15[1]);
        waitW(17);
        waitK(32);
        tr_chunk(tbk, 7 * SLOT, vf);
        OPAQUE(tb0);
        v4u fa[2][4], fb[2][4];
        SB();
#pragma unroll
        for (int g = 0; g < 8; ++g) {
            tr_frag_g(tb0, 0, g, fa);
            acc[g >> 1] = mfma32(vf[g & 1][g >> 1], p15[g & 1], acc[g >> 1]);
            SB();
        }
        STAMP(4);
        v4u cur[8];
#pragma unroll
        for (int d = 0; d < 4; ++d) {
            acc[d] *= inv; cur[2 * d] = pack8(acc[d], 0); cur[2 * d + 1] = pack8(acc[d], 1);
            __builtin_amdgcn_raw_buffer_store_b128(cur[2 * d], rws, lane * 16 + (2 * d) * 1024, (int)WS_O + wgid * 24576 + t * 8192, 16);
            __builtin_amdgcn_raw_buffer_store_b128(cur[2 * d + 1], rws, lane * 16 + (2 * d + 1) * 1024, (int)WS_O + wgid * 24576 + t * 8192, 16);
        }
#pragma unroll
        for (int d = 0; d < 4; ++d) acc[d] = f32x16{};
        STAMP(5);
        auto gemm4 = [&](int c0, const v4u (&cur)[8]) {
            const int last = c0 + 4 < 16 ? c0 + 4 : 15;
            waitW(min(17 + last, 16 + NWO1));
            if (last >= NWO1) waitK(33 + last - NWO1);
            unsigned tb = tb0 + c0 * SLOT;
            OPAQUE(tb);
            SB();
            gemm_step<true>(acc, fa, cur[0], cur[1], tb, SLOT, fb);
            gemm_step<true>(acc, fb, cur[2], cur[3], tb, 2 * SLOT, fa);
            gemm_step<true>(acc, fa, cur[4], cur[5], tb, 3 * SLOT, fb);
            if (c0 < 12) gemm_step<true>(acc, fb, cur[6], cur[7], tb, 4 * SLOT, fa);
            else gemm_step<false>(acc, fb, cur[6], cur[7], 0, 0, fa);
        };
        auto ld8 = [&](int s) {
#pragma unroll
            for (int k = 0; k < 8; ++k) pf[s][k] = __builtin_amdgcn_raw_buffer_load_b128(rws, lane * 16 + k * 1024, pso[s], 16);
        };
        gemm4(0, cur);
        VMCNT(0);
        if (lane == 0) __hip_atomic_store((GAS u64*)(ws + WS_F1 + (wgid * 3 + t) * 128), tag, RLX_AGENT);
        STAMP(6);
        {
            int ps = (lane & 3) + rotp; ps = ps >= 3 ? ps - 3 : ps;
            const int pw = b * 32 + xcd * 4 + ((lane & 3) == 3 ? hl : ((hl + 1 + ps) & 3));
            GAS u64* fp = (GAS u64*)(ws + WS_F1 + (pw * 3 + t) * 128);
            unsigned got = 0;
            auto poll = [&]() -> unsigned { const u64 f = __hip_atomic_load(fp, RLX_AGENT); const unsigned r = (unsigned)__builtin_amdgcn_ballot_w64(f == tag) & 7u; CBAR(); return r; };
            unsigned rdy = 0;
            for (unsigned sp = 0; !(rdy & 1u) && sp < SPIN_CAP; ++sp) rdy = poll();
            ld8(0);
            if (rdy & 2u) { ld8(1); got |= 2u; }
            if (rdy & 4u) { ld8(2); got |= 4u; }
            STAMP(11);
            if (got != 6u) {
                rdy = poll();
                if (!(got & 2u) && (rdy & 2u)) { ld8(1); got |= 2u; }
                if (!(got & 4u) && (rdy & 4u)) { ld8(2); got |= 4u; }
            }
            gemm4(4, pf[0]);
            if (!(got & 2u)) {
                for (unsigned sp = 0; !(rdy & 2u) && sp < SPIN_CAP; ++sp) rdy = poll();
                ld8(1);
                if (!(got & 4u) && (rdy & 4u)) { ld8(2); got |= 4u; }
            }
            STAMP(12);
            gemm4(8, pf[1]);
            if (!(got & 4u)) {
                for (unsigned sp = 0; !(rdy & 4u) && sp < SPIN_CAP; ++sp) rdy = poll();
                ld8(2);
            }
            STAMP(13);
            gemm4(12, pf[2]);
        }
        STAMP(7);
#pragma unroll
        for (int d = 0; d < 4; ++d) {
            __builtin_amdgcn_raw_buffer_store_b128(pack8(acc[d], 0), rws, lane * 16 + (2 * d) * 1024, (int)WS_P + wgid * 24576 + t * 8192, 16);
            __builtin_amdgcn_raw_buffer_store_b128(pack8(acc[d], 1), rws, lane * 16 + (2 * d + 1) * 1024, (int)WS_P + wgid * 24576 + t * 8192, 16);
        }
        ctl[C_DONE / 4] = 1;
        VMCNT(0);
        if (lane < 2) __hip_atomic_store((GAS u64*)(ws + WS_F2 + ((wgid * 3 + t) * 2 + lane) * 128), tag, RLX_AGENT);
        STAMP(8);
    }
    if (xcd < 6 && w < 4) {
        const int t = xcd >> 1, f = 4 * (xcd & 1) + w;
        if (w == 3) {
            for (unsigned sp = 0; rfl(lds_ld(Lb + CTL + C_DONE)) == 0u && sp < SPIN_CAP; ++sp) __builtin_amdgcn_s_sleep(2);
            GAS u64* fp = (GAS u64*)(ws + WS_F2 + (((b * 32 + (lane & 7) * 4 + hl) * 3 + t) * 2 + (xcd & 1)) * 128);
            for (unsigned sp = 0; sp < SPIN_CAP; ++sp) {
                const u64 fl = __hip_atomic_load(fp, RLX_AGENT);
                if (__builtin_amdgcn_ballot_w64(fl != tag) == 0ull) break;
            }
            lds_st(Lb + CTL + C_READY, 1);
            if (xcd == 0 && hl == 0 && lane == 0) __hip_atomic_store((GAS u64*)(ws + WS_EPOCH + b * 256), tag, RLX_AGENT);
        } else {
            for (unsigned sp = 0; rfl(lds_ld(Lb + CTL + C_READY)) == 0u && sp < SPIN_CAP; ++sp) {}
        }
        CBAR();
        STAMP(9);
        v4u pp[8];
#pragma unroll
        for (int xx = 0; xx < 8; ++xx) pp[xx] = __builtin_amdgcn_raw_buffer_load_b128(rws, lane * 16, (int)WS_P + (b * 32 + xx * 4 + hl) * 24576 + t * 8192 + f * 1024, 16);
        float s[8];
#pragma unroll
        for (int k = 0; k < 8; ++k) s[k] = 0.f;
#pragma unroll
        for (int xx = 0; xx < 8; ++xx)
#pragma unroll
            for (int k = 0; k < 4; ++k) { s[2 * k] += __uint_as_float(pp[xx][k] << 16); s[2 * k + 1] += __uint_as_float(pp[xx][k] & 0xffff0000u); }
        v4u o = {cvtpk(s[0], s[1]), cvtpk(s[2], s[3]), cvtpk(s[4], s[5]), cvtpk(s[6], s[7])};
        *(v4u*)((char*)out + ((size_t)(b * 96 + 32 * t + (lane & 31)) * 512 + 128 * hl + 16 * f + 8 * (lane >> 5)) * 2) = o;
        STAMP(10);
    }
    STAMP_FLUSH(ws);
}

extern "C" void kernel_launch(void* const* d_in, const int* in_sizes, int n_in, void* d_out, int out_size, void* d_ws, size_t ws_size, hipStream_t stream) {
    (void)hipFuncSetAttribute((const void*)fused_kernel, hipFuncAttributeMaxDynamicSharedMemorySize, LDS_BYTES);
    fused_kernel<<<256, 320, LDS_BYTES, stream>>>(d_in[0], d_in[1], d_in[2], d_in[3], d_in[4], d_out, (char*)d_ws);
}
```

```cpp
#include <hip/hip_runtime.h>
#include <hip/hip_bf16.h>
#include <type_traits>

typedef unsigned v4u __attribute__((ext_vector_type(4)));
typedef unsigned v2u __attribute__((ext_vector_type(2)));
typedef float f32x16 __attribute__((ext_vector_type(16)));
typedef float f32x2 __attribute__((ext_vector_type(2)));
typedef __bf16 bf16x8 __attribute__((ext_vector_type(8)));
typedef __bf16 bf16x2 __attribute__((ext_vector_type(2)));
typedef short s16x4 __attribute__((ext_vector_type(4)));
typedef unsigned long long u64;
#define LAS __attribute__((address_space(3)))
#define GAS __attribute__((address_space(1)))
#define DI __device__ __forceinline__
#define RLX_AGENT __ATOMIC_RELAXED, __HIP_MEMORY_SCOPE_AGENT

#define KV_AUX 2
#define KV_D 4
#define W_D 3
#define CLR_SLEEP 4
#define LD_SLEEP 1
#define CP_SLEEP 1
#define KV_PRE 0
#define X_D 3
#define LD_PRIO 3
#define TROT idx
#define F1_EARLY 1
#define P_SLEEP 0
#define CODE_PF 35840
#define XS1 3
#define XS2 5
#define XS3 9
#define TYPEB 0
#define WO_START 24
#define WO_D 3
#define KV_START 4

#define STAMP(i)
#define STAMP_DECL
#define STAMP_FLUSH(ws)

constexpr int SLOT = 8192;
constexpr int P_BASE = 0, X_BASE = 8 * SLOT, S_BASE = 11 * SLOT, CTL = 19 * SLOT;
constexpr int LDS_BYTES = CTL + 512;
constexpr int C_FULLK = 0, C_FULLW = 4, C_G = 16  , C_READY = 64, C_DONE = 68;
constexpr size_t WS_EPOCH = 0;
constexpr size_t WS_F1 = 4096;
constexpr size_t WS_F2 = WS_F1 + 768 * 128;
constexpr size_t WS_O = WS_F2 + 1536 * 128;
constexpr size_t WS_P = WS_O + 256 * 24576;
constexpr int WS_BYTES = (int)(WS_P + 256 * 24576);
constexpr unsigned SPIN_CAP = 1u << 22;

DI f32x16 mfma32(v4u a, v4u b, f32x16 c) {
    return __builtin_amdgcn_mfma_f32_32x32x16_bf16(__builtin_bit_cast(bf16x8, a), __builtin_bit_cast(bf16x8, b), c, 0, 0, 0);
}
DI unsigned cvtpk(float lo, float hi) { f32x2 v = {lo, hi}; bf16x2 b = __builtin_convertvector(v, bf16x2); return __builtin_bit_cast(unsigned, b); }
DI __amdgpu_buffer_rsrc_t mkrsrc(const void* p, int bytes) { return __builtin_amdgcn_make_buffer_rsrc((void*)p, 0, bytes, 0x00020000); }
DI unsigned rfl(unsigned x) { return __builtin_amdgcn_readfirstlane(x); }
DI v4u pack8(const f32x16& a, int s) {
    return v4u{cvtpk(a[8 * s], a[8 * s + 1]), cvtpk(a[8 * s + 2], a[8 * s + 3]), cvtpk(a[8 * s + 4], a[8 * s + 5]), cvtpk(a[8 * s + 6], a[8 * s + 7])};
}
DI float swapmax(float x) { auto r = __builtin_amdgcn_permlane32_swap(__float_as_uint(x), __float_as_uint(x), false, false); return fmaxf(__uint_as_float(r[0]), __uint_as_float(r[1])); }
DI float swapsum(float x) { auto r = __builtin_amdgcn_permlane32_swap(__float_as_uint(x), __float_as_uint(x), false, false); return __uint_as_float(r[0]) + __uint_as_float(r[1]); }
DI float max3f(float a, float b, float c) { float r; asm("v_max3_f32 %0, %1, %2, %3" : "=v"(r) : "v"(a), "v"(b), "v"(c)); return r; }
DI float fadd_s(float a, float b) { float r; asm("v_add_f32_e32 %0, %1, %2" : "=v"(r) : "v"(a), "v"(b)); return r; }
#define SB() __builtin_amdgcn_sched_barrier(0)
#define OPAQUE(x) asm volatile("" : "+v"(x))
#define CBAR() asm volatile("" ::: "memory")
#define VMCNT(n) asm volatile("s_waitcnt vmcnt(%0)" :: "i"(n) : "memory")
#define LGKM0() asm volatile("s_waitcnt lgkmcnt(0)" ::: "memory")

DI unsigned lds_ld(unsigned addr) { unsigned r; asm volatile("ds_read_b32 %0, %1\n\ts_waitcnt lgkmcnt(0)" : "=v"(r) : "v"(addr) : "memory"); return r; }
DI void lds_st(unsigned addr, unsigned v) { asm volatile("ds_write_b32 %0, %1" :: "v"(addr), "v"(v) : "memory"); }
DI unsigned lds_min3(unsigned addr) {
    unsigned a, b, c;
    asm volatile("ds_read_b32 %0, %3\n\tds_read_b32 %1, %3 offset:4\n\tds_read_b32 %2, %3 offset:8\n\ts_waitcnt lgkmcnt(0)" : "=&v"(a), "=&v"(b), "=&v"(c) : "v"(addr) : "memory");
    return rfl(min(a, min(b, c)));
}

template <int AUX> DI void dma_chunk(__amdgpu_buffer_rsrc_t rs, LAS char* slot, const int (&voff)[4], int soff, int half) {
    __builtin_amdgcn_raw_ptr_buffer_load_lds(rs, (LAS void*)slot, 16, voff[0], soff, 0, AUX);
    __builtin_amdgcn_raw_ptr_buffer_load_lds(rs, (LAS void*)slot, 16, voff[1], soff, 1024, AUX);
    __builtin_amdgcn_raw_ptr_buffer_load_lds(rs, (LAS void*)slot, 16, voff[2], soff, 2048, AUX);
    __builtin_amdgcn_raw_ptr_buffer_load_lds(rs, (LAS void*)slot, 16, voff[3], soff, 3072, AUX);
    __builtin_amdgcn_raw_ptr_buffer_load_lds(rs, (LAS void*)(slot + 4096), 16, voff[0], soff + half, 0, AUX);
    __builtin_amdgcn_raw_ptr_buffer_load_lds(rs, (LAS void*)(slot + 4096), 16, voff[1], soff + half, 1024, AUX);
    __builtin_amdgcn_raw_ptr_buffer_load_lds(rs, (LAS void*)(slot + 4096), 16, voff[2], soff + half, 2048, AUX);
    __builtin_amdgcn_raw_ptr_buffer_load_lds(rs, (LAS void*)(slot + 4096), 16, voff[3], soff + half, 3072, AUX);
}
DI v2u tr_rd(unsigned addr, int off) { return __builtin_bit_cast(v2u, __builtin_amdgcn_ds_read_tr16_b64_v4i16((LAS s16x4*)(size_t)(addr + off))); }
DI v4u row_rd(unsigned addr) { return *(LAS v4u*)(size_t)addr; }
DI v4u tr_frag(unsigned tb, int off, int s, int ct) {
    v2u l2 = tr_rd(tb ^ (unsigned)(64 * ct), off + s * 4096), h2 = tr_rd(tb ^ (unsigned)(64 * ct + 16), off + s * 4096 + 1024);
    return v4u{l2[0], l2[1], h2[0], h2[1]};
}
DI void tr_frag_g(unsigned tb, int off, int g, v4u (&fr)[2][4]) { fr[g & 1][g >> 1] = tr_frag(tb, off, g & 1, g >> 1); }
DI void tr_chunk(unsigned tb, int off, v4u (&fr)[2][4]) {
#pragma unroll
    for (int g = 0; g < 8; ++g) tr_frag_g(tb, off, g, fr);
}
DI void tro_frag_g(unsigned tb, int off, int g, v4u (&fr)[2][4]) {
    const unsigned a = tb ^ (unsigned)(32 * (g >> 1));
    v2u l2 = tr_rd(a, off + (g & 1) * 4096), h2 = tr_rd(a, off + (g & 1) * 4096 + 1024);
    fr[g & 1][g >> 1] = v4u{l2[0], l2[1], h2[0], h2[1]};
}
template <bool NEXT> DI void gemm_step(f32x16 (&acc)[4], const v4u (&cur)[2][4], v4u b0, v4u b1, unsigned tb, int off, v4u (&nxt)[2][4]) {
#pragma unroll
    for (int g = 0; g < 8; ++g) {
        acc[g >> 1] = mfma32(cur[g & 1][g >> 1], (g & 1) ? b1 : b0, acc[g >> 1]);
        if (NEXT) tro_frag_g(tb, off, g, nxt);
        SB();
    }
}

__global__ __launch_bounds__(320, 2) void fused_kernel(const void* __restrict__ x, const void* __restrict__ K, const void* __restrict__ V,
                                                      const void* __restrict__ wq, const void* __restrict__ wo, void* __restrict__ out, char* ws) {
    extern __shared__ __attribute__((aligned(16))) char smem[];
    LAS char* L = (LAS char*)smem;
    const unsigned Lb = (unsigned)(size_t)L;
    const int tid = threadIdx.x, lane = tid & 63, w = rfl(tid >> 6);
    const int xcd = blockIdx.x & 7, idx = blockIdx.x >> 3;
    const int hl = idx & 3, b = idx >> 2, h = xcd * 4 + hl, bh = b * 32 + h;
    const int wgid = bh;
    STAMP_DECL
    STAMP(0);
    const __amdgpu_buffer_rsrc_t rws = mkrsrc(ws, WS_BYTES);
    const int cb0 = ((b >> 1) + hl) & 3, rotp = b % 3;
    u64 tag = 0;
    constexpr int npre = 0;
    const int pos = lane & 15, rq = lane >> 4;
    auto wq_off = [&](int n) -> int { return ((((n >> 2) + cb0) & 3) * 128 + ((n & 3) ^ (2 * (b & 1))) * 32) * 8192 + h * 256; };
    {
        const unsigned long long pc = __builtin_amdgcn_s_getpc() & ~255ull;
        __builtin_amdgcn_raw_ptr_buffer_load_lds(mkrsrc((const void*)pc, CODE_PF), (LAS void*)(L + CTL + 256), 4, lane * 4, (idx * 5 + w) * 256, 0, 0);
    }
    {
        const bool isx = w < 3;
        const int sh = isx ? 10 : 13;
        const __amdgpu_buffer_rsrc_t r0 = mkrsrc(isx ? (const char*)x + (size_t)b * 98304 : (const char*)wq, isx ? 98304 : 512 * 8192);
        int vo[4];
#pragma unroll
        for (int k = 0; k < 4; ++k) vo[k] = ((4 * k + rq) << sh) + 16 * (pos ^ (rq << 2) ^ k) - 1024 * k;
        dma_chunk<0>(r0, L + X_BASE + w * SLOT, vo, isx ? w * 32768 + cb0 * 256 : wq_off(w - 3), 16 << sh);
    }

    if (w >= 3) {
        int vo1k[4];
#pragma unroll
        for (int k = 0; k < 4; ++k) vo1k[k] = (4 * k + rq) * 1024 + 16 * (pos ^ (rq << 2) ^ k) - 1024 * k;
        const __amdgpu_buffer_rsrc_t rwo = mkrsrc(wo, 4096 * 1024);
        auto wo_off = [&](int ci) -> int {
            int head = hl;
            if (ci >= 4) { int ps = (ci >> 2) - 1 + rotp; ps = ps >= 3 ? ps - 3 : ps; head = (hl + 1 + ps) & 3; }
            return ((xcd * 4 + head) * 128 + (ci & 3) * 32) * 1024 + hl * 256;
        };
        __builtin_amdgcn_s_setprio(LD_PRIO);
        unsigned prog = 0;
        unsigned fulla = Lb + CTL + (w == 3 ? C_FULLK : C_FULLW);
        OPAQUE(fulla);
        auto gate = [&](unsigned need) {
            if (__builtin_expect(prog < need, 0)) {
                for (unsigned sp = 0; prog < need && sp < SPIN_CAP; ++sp) { if (sp) __builtin_amdgcn_s_sleep(LD_SLEEP); prog = lds_min3(Lb + CTL + C_G); }
            }
        };
        if (w == 3) {
            const __amdgpu_buffer_rsrc_t rx = mkrsrc((const char*)x + (size_t)b * 98304, 98304);
            const __amdgpu_buffer_rsrc_t rk = mkrsrc((const char*)K + (size_t)bh * 131072, 131072);
            const __amdgpu_buffer_rsrc_t rv = mkrsrc((const char*)V + (size_t)bh * 131072, 131072);
            int vo256[4];
#pragma unroll
            for (int k = 0; k < 4; ++k) vo256[k] = rq * 256 + 16 * (pos ^ (rq << 2) ^ k);
            const int trot = TROT;
            tag = __hip_atomic_load((GAS u64*)(ws + WS_EPOCH + b * 256), RLX_AGENT) + 1;
#pragma unroll
            for (int i = 3; i < 12; ++i) {
                const int cbi = i / 3, r = i % 3;
                if (i >= 3 + X_D) { VMCNT(8 * (X_D - 1)); lds_st(fulla, npre + i - X_D + 1); }
                if (i == 9) gate(1);
                dma_chunk<0>(rx, L + (cbi == 1 ? P_BASE + 2 * SLOT : (cbi == 2 ? P_BASE + 5 * SLOT : X_BASE)) + r * SLOT, vo1k, r * 32768 + ((cbi + cb0) & 3) * 256, 16384);
                if (i == 3 + X_D - 1) { VMCNT(8 * X_D); __builtin_amdgcn_s_barrier(); OPAQUE(tag); }
            }
            unsigned pubd = npre + 12 - X_D;
            prog = lds_min3(Lb + CTL + C_G);
            if (prog < KV_START) {
#pragma unroll
                for (int d = X_D - 1; d >= 0; --d) { VMCNT(8 * d); lds_st(fulla, npre + 12 - d); }
                pubd = npre + 12;
                gate(KV_START);
            }
            STAMP(1);
#pragma unroll 1
            for (int p = npre >> 1; p < 16; ++p) {
                const int c = 2 * p;
                LAS char* sl = L + ((c & 8) ? S_BASE : P_BASE) + (c & 7) * SLOT;
                VMCNT(8 * (KV_D - 1)); lds_st(fulla, max(pubd, (unsigned)(12 + c - KV_D + 1)));
                if (c >= 8) gate(c + 1);
                else if (p == 1) gate(XS1 + 2);
                if (p > 0) dma_chunk<KV_AUX>(rv, sl, vo256, ((p - 1 + trot) & 15) * 8192, 4096);
                else dma_chunk<KV_AUX>(rk, sl, vo256, (trot & 15) * 8192, 4096);
                VMCNT(8 * (KV_D - 1)); lds_st(fulla, max(pubd, (unsigned)(12 + c + 1 - KV_D + 1)));
                if (c >= 8) gate(c + 2);
                else if (p == 2) gate(XS2 + 2);
                if (p < 15) dma_chunk<KV_AUX>(rk, sl + SLOT, vo256, ((p + 1 + trot) & 15) * 8192, 4096);
                else dma_chunk<KV_AUX>(rv, sl + SLOT, vo256, ((15 + trot) & 15) * 8192, 4096);
            }
            STAMP(2);
#pragma unroll
            for (int d = KV_D - 1; d >= 0; --d) { VMCNT(8 * d); lds_st(fulla, 12 + 32 - d); }
            STAMP(3);
        } else {
            const __amdgpu_buffer_rsrc_t rwq = mkrsrc(wq, 512 * 8192);
            int vo8k[4];
#pragma unroll
            for (int k = 0; k < 4; ++k) vo8k[k] = (4 * k + rq) * 8192 + 16 * (pos ^ (rq << 2) ^ k) - 1024 * k;
#pragma unroll
            for (int n = 2; n < 16; ++n) {
                if (n >= 2 + W_D) { VMCNT(8 * (W_D - 1)); lds_st(fulla, n - W_D + 1); }
                if (n >= 8) gate(n - 7);
                dma_chunk<0>(rwq, L + S_BASE + (n & 7) * SLOT, vo8k, wq_off(n), 131072);
                if (n == 3) { VMCNT(16); __builtin_amdgcn_s_barrier(); }
            }
            STAMP(1);
#pragma unroll
            for (int d = W_D - 1; d >= 0; --d) { VMCNT(8 * d); lds_st(fulla, 16 - d); }
            int vow[4];
#pragma unroll
            for (int k = 0; k < 4; ++k) vow[k] = (4 * k + rq) * 1024 + 16 * (pos ^ (rq << 1)) - 1024 * k;
#pragma unroll 1
            for (int i = 0; i < 16; ++i) {
                const int ci = i < 3 ? 8 + i : (i < 11 ? i - 3 : i);
                gate(i < 3 ? WO_START : 30 + i);
                dma_chunk<0>(rwo, L + ci * SLOT, vow, wo_off(ci), 16384);
                VMCNT(8 * (WO_D - 1)); if (i + 2 > WO_D) lds_st(fulla, 16 + i + 2 - WO_D);
            }
#pragma unroll
            for (int d = WO_D - 2; d >= 0; --d) { VMCNT(8 * d); lds_st(fulla, 32 - d); }
            STAMP(2);
        }
    } else {
        const int t = w;
        if (tid < 64) lds_st(Lb + CTL + tid * 4, 0u);
        tag = __hip_atomic_load((GAS u64*)(ws + WS_EPOCH + b * 256), RLX_AGENT) + 1;
        const int r32 = lane & 31, hh = lane >> 5;
        volatile LAS unsigned* ctl = (volatile LAS unsigned*)(L + CTL);
        const int fr_ = ((r32 & 3) << 2) | ((r32 >> 2) & 3);
        unsigned rbx = Lb + t * SLOT + ((256 * r32 + 16 * (hh ^ fr_)) ^ (128 * (b & 1)));
        const int pr = (r32 & 19) | ((r32 & 4) << 1) | ((r32 & 8) >> 1);
        const int fp_ = ((pr & 3) << 2) | ((pr >> 2) & 3);
        unsigned rbk = Lb + P_BASE + 256 * pr + 16 * (hh ^ fp_);
        const int blk = (lane >> 4) & 1, q4 = (lane & 15) >> 2, p4 = lane & 3;
        unsigned tb0 = Lb + 256 * (8 * hh + q4) + 64 * q4 + 16 * ((2 * blk + (p4 & 1)) ^ (2 * hh)) + 8 * (p4 >> 1);
        unsigned availW = 2, availK = 0;
        auto waitW = [&](unsigned need) { if (__builtin_expect(availW < need, 0)) for (unsigned sp = 0; availW < need && sp < SPIN_CAP; ++sp) { if (sp) __builtin_amdgcn_s_sleep(CP_SLEEP); availW = rfl(ctl[C_FULLW / 4]); } CBAR(); };
        auto waitKs = [&](unsigned need) { if (__builtin_expect(availK < need, 0)) for (unsigned sp = 0; availK < need && sp < SPIN_CAP; ++sp) { if (sp) __builtin_amdgcn_s_sleep(CP_SLEEP); availK = rfl(ctl[C_FULLK / 4]); } CBAR(); };
        auto waitK = [&](unsigned n) { waitKs(12 + n); };
        f32x16 acc[4];
#pragma unroll
        for (int d = 0; d < 4; ++d) acc[d] = f32x16{};
        {
            v4u fa[2][4], fb[2][4], xf[32];
            const unsigned tbw = tb0 + S_BASE;
            asm volatile("s_waitcnt vmcnt(0) lgkmcnt(0)" ::: "memory");
            __builtin_amdgcn_s_barrier();
            CBAR();
            STAMP(14);
            xf[0] = row_rd(rbx + X_BASE); xf[1] = row_rd((rbx + X_BASE) ^ 32u);
            tr_chunk(tbw, 0, fa);
#pragma unroll
            for (int ks = 2; ks < 8; ++ks) xf[ks] = row_rd(((rbx + X_BASE) ^ (unsigned)(32 * ks)));
            ctl[C_G / 4 + t] = 1;
#pragma unroll
            for (int n = 0; n < 16; ++n) {
                const int xq = n == XS1 ? 1 : (n == XS2 ? 2 : (n == XS3 ? 3 : 0));
                if (n & 1) waitW(n + 3 < 16 ? n + 3 : 16);
                if (xq) waitKs(npre + 3 * xq + t + 1);
                const int offn = ((n + 1) & 7) * SLOT;
                const unsigned xn = rbx + (xq == 1 ? P_BASE + 2 * SLOT : (xq == 2 ? P_BASE + 5 * SLOT : X_BASE));
                v2u fw;
                SB();
#pragma unroll
                for (int g = 0; g < 8; ++g) {
                    if (n & 1) { acc[g >> 1] = mfma32(fb[g & 1][g >> 1], xf[2 * n + (g & 1)], acc[g >> 1]); if (n < 15) tr_frag_g(tbw, offn, g, fa); }
                    else { acc[g >> 1] = mfma32(fa[g & 1][g >> 1], xf[2 * n + (g & 1)], acc[g >> 1]); tr_frag_g(tbw, offn, g, fb); }
                    if (xq) xf[8 * xq + g] = row_rd(xn ^ (unsigned)(32 * g));
                    if (g == 3 && !(n & 1)) fw = *(volatile LAS v2u*)(L + CTL);
                    if (g == 0 && !(n & 1) && n > 0) ctl[C_G / 4 + t] = n + 1;
                    if (g == 7 && n == 14) ctl[C_G / 4 + t] = 16;
                    if (g == 7 && !(n & 1)) { availK = rfl(fw[0]); availW = max(2u, rfl(fw[1])); }
                    SB();
                }
            }
        }
        asm volatile("" :: "v"(tag));
        STAMP(1);
        constexpr float CS = 0.08838834764831845f * 1.4426950408889634f;
        constexpr float THR = 8.0f;
        v4u qf[8];
#pragma unroll
        for (int d = 0; d < 4; ++d) { acc[d] *= CS; qf[2 * d] = pack8(acc[d], 0); qf[2 * d + 1] = pack8(acc[d], 1); }
#pragma unroll
        for (int d = 0; d < 4; ++d) acc[d] = f32x16{};
        float lsum = 0.f;
        f32x16 sA, sB, negm; v4u kf[8], vf[2][4];
        unsigned tbk = tb0 + P_BASE; OPAQUE(tbk);
        unsigned ctla = Lb + CTL + C_FULLK, pka = Lb + CTL + C_G + 4 * t, relv = 18;
        OPAQUE(ctla); OPAQUE(pka); OPAQUE(relv);
        auto decide = [&](float tm, f32x16& nxt) {
            if (__builtin_expect(__builtin_amdgcn_ballot_w64(tm > THR) != 0ull, 0)) {
                const float dl = fmaxf(swapmax(tm), 0.f), f = __builtin_amdgcn_exp2f(-dl);
                lsum *= f;
#pragma unroll
                for (int d = 0; d < 4; ++d) acc[d] *= f;
#pragma unroll
                for (int i = 0; i < 16; ++i) { nxt[i] -= dl; negm[i] -= dl; }
            }
        };
        auto block = [&](f32x16& cur, f32x16& nxt, int vslot, auto KR) -> float {
            v4u p[2];
            float m = 0.f, m1 = 0.f, s0 = 0.f, s1 = 0.f; unsigned fk = 0;
            SB();
#pragma unroll
            for (int g = 0; g < 8; ++g) {
                nxt = mfma32(kf[g], qf[g], g == 0 ? negm : nxt);
                tr_frag_g(tbk, vslot * SLOT, g, vf);
                const int e0 = (3 * g + 1) / 2, e1 = (3 * (g + 1) + 1) / 2;
#pragma unroll
                for (int e = e0; e < e1; ++e) cur[e] = __builtin_amdgcn_exp2f(cur[e]);
                if (g >= 1) {
                    const int pp0 = ((3 * (g - 1) + 1) / 2) / 2, pp1 = e0 / 2;
#pragma unroll
                    for (int q = pp0; q < pp1; ++q) p[q >> 2][q & 3] = cvtpk(cur[2 * q], cur[2 * q + 1]);
                }
                SB();
            }
#pragma unroll
            for (int g = 0; g < 8; ++g) {
                acc[g & 3] = mfma32(vf[g >> 2][g & 3], p[g >> 2], acc[g & 3]);
                if (KR && g < 4) { kf[2 * g] = row_rd((rbk ^ (unsigned)(64 * g)) + (vslot + 1) * SLOT); kf[2 * g + 1] = row_rd((rbk ^ (unsigned)(64 * g + 32)) + (vslot + 1) * SLOT); }
                if (g == 0) { cur[12] = __builtin_amdgcn_exp2f(cur[12]); cur[13] = __builtin_amdgcn_exp2f(cur[13]); p[1][1] = cvtpk(cur[10], cur[11]); }
                if (g == 1) { cur[14] = __builtin_amdgcn_exp2f(cur[14]); cur[15] = __builtin_amdgcn_exp2f(cur[15]); p[1][2] = cvtpk(cur[12], cur[13]); }
                if (g == 2) { p[1][3] = cvtpk(cur[14], cur[15]); m = max3f(nxt[0], nxt[1], nxt[2]); m1 = max3f(nxt[3], nxt[4], nxt[5]); }
                if (g == 3) { m = max3f(m, nxt[6], nxt[7]); m1 = max3f(m1, nxt[8], nxt[9]); }
                if (g == 4) { m = max3f(m, nxt[10], nxt[11]); m1 = max3f(m1, nxt[12], nxt[13]); relv += KR ? 2 : 1; *(volatile LAS unsigned*)(size_t)pka = relv; fk = *(volatile LAS unsigned*)(size_t)ctla; }
                if (g == 5) { m = max3f(m, nxt[14], nxt[15]); m = max3f(m, m1, m1); }
                if (g >= 4) {
#pragma unroll
                    for (int e = 4 * (g - 4); e < 4 * (g - 4) + 4; e += 2) { s0 = fadd_s(s0, cur[e]); s1 = fadd_s(s1, cur[e + 1]); }
                }
                SB();
            }
            lsum += s0 + s1;
            OPAQUE(lsum);
            availK = rfl(fk);
            return m;
        };
        waitK(1);
#pragma unroll
        for (int ks = 0; ks < 8; ++ks) kf[ks] = row_rd(rbk ^ (unsigned)(32 * ks));
#pragma unroll
        for (int ks = 0; ks < 8; ++ks) sA = mfma32(kf[ks], qf[ks], ks == 0 ? f32x16{} : sA);
        waitK(2);
#pragma unroll
        for (int ks = 0; ks < 8; ++ks) kf[ks] = row_rd((rbk ^ (unsigned)(32 * ks)) + SLOT);
        *(volatile LAS unsigned*)(size_t)pka = relv;
        {
            float m = fmaxf(sA[0], sA[1]);
#pragma unroll
            for (int i = 2; i < 16; i += 2) m = fmaxf(fmaxf(m, sA[i]), sA[i + 1]);
            m = swapmax(m);
#pragma unroll
            for (int i = 0; i < 16; ++i) { sA[i] -= m; negm[i] = -m; }
            OPAQUE(negm);
        }
        STAMP(2);
        {
            constexpr std::integral_constant<bool, true> T{};
            constexpr std::integral_constant<bool, false> F{};
#pragma unroll 1
            for (int j = 0; j < 12; j += 4) {
                waitK(2 * j + 4); decide(block(sA, sB, 2, T), sB);
                waitK(2 * j + 6); decide(block(sB, sA, 4, T), sA);
                waitK(2 * j + 8); decide(block(sA, sB, 6, T), sB);
                tbk ^= S_BASE; rbk ^= S_BASE;
                waitK(2 * j + 10); decide(block(sB, sA, 0, T), sA);
            }
            waitK(28); decide(block(sA, sB, 2, T), sB);
            waitK(30); decide(block(sB, sA, 4, T), sA);
            waitK(31);
            decide(block(sA, sB, 6, F), sB);
            STAMP(3);
        }
        v4u pf[3][8];
        int pso[3];
#pragma unroll
        for (int s = 0; s < 3; ++s) {
            int ps = s + rotp; ps = ps >= 3 ? ps - 3 : ps;
            pso[s] = (int)WS_O + (b * 32 + xcd * 4 + ((hl + 1 + ps) & 3)) * 24576 + t * 8192;
        }
        v4u p15[2];
        {
            float s0 = 0.f, s1 = 0.f;
#pragma unroll
            for (int g = 0; g < 8; ++g) {
                const float e0 = __builtin_amdgcn_exp2f(sB[2 * g]), e1 = __builtin_amdgcn_exp2f(sB[2 * g + 1]);
                s0 += e0; s1 += e1; p15[g >> 2][g & 3] = cvtpk(e0, e1);
            }
            lsum += s0 + s1;
        }
        float inv = 1.0f / swapsum(lsum);
        OPAQUE(inv); OPAQUE(p15[0]); OPAQUE(p15[1]);
        waitW(20);
        waitK(32);
        tr_chunk(tbk, 7 * SLOT, vf);
        unsigned tbo = Lb + 256 * (8 * hh + q4) + 16 * ((8 * (p4 & 1) + blk) ^ (q4 << 1)) + 8 * (p4 >> 1);
        OPAQUE(tbo);
        v4u fa[2][4], fb[2][4];
        SB();
#pragma unroll
        for (int g = 0; g < 8; ++g) {
            tro_frag_g(tbo, 0, g, fa);
            acc[g >> 1] = mfma32(vf[g & 1][g >> 1], p15[g & 1], acc[g >> 1]);
            SB();
        }
        STAMP(4);
        const int l16 = lane * 16;
        v4u cur[8];
        {
            const __amdgpu_buffer_rsrc_t ro = mkrsrc(ws + WS_O + wgid * 24576 + t * 8192, 8192);
#pragma unroll
            for (int d = 0; d < 4; ++d) {
                acc[d] *= inv; cur[2 * d] = pack8(acc[d], 0); cur[2 * d + 1] = pack8(acc[d], 1);
                __builtin_amdgcn_raw_buffer_store_b128(cur[2 * d], ro, l16 + (2 * d) * 1024, 0, 16);
                __builtin_amdgcn_raw_buffer_store_b128(cur[2 * d + 1], ro, l16 + (2 * d + 1) * 1024, 0, 16);
            }
        }
#pragma unroll
        for (int d = 0; d < 4; ++d) acc[d] = f32x16{};
        STAMP(5);
        auto gemm4 = [&](int c0, const v4u* cur) {
            waitW(c0 == 0 ? 24 : (c0 == 4 ? 27 : (c0 == 8 ? 29 : 32)));
            unsigned tb = tbo + c0 * SLOT;
            OPAQUE(tb);
            SB();
            gemm_step<true>(acc, fa, cur[0], cur[1], tb, SLOT, fb);
            gemm_step<true>(acc, fb, cur[2], cur[3], tb, 2 * SLOT, fa);
            if (c0 == 0 && F1_EARLY) {
                VMCNT(0);
                if (lane == 0) __hip_atomic_store((GAS u64*)(ws + WS_F1 + (wgid * 3 + t) * 128), tag, RLX_AGENT);
                SB();
            }
            gemm_step<true>(acc, fa, cur[4], cur[5], tb, 3 * SLOT, fb);
            if (c0 < 12) gemm_step<true>(acc, fb, cur[6], cur[7], tb, 4 * SLOT, fa);
            else gemm_step<false>(acc, fb, cur[6], cur[7], 0, 0, fa);
        };
        auto ld8 = [&](int s) {
#pragma unroll
            for (int k = 0; k < 8; ++k) pf[s][k] = __builtin_amdgcn_raw_buffer_load_b128(rws, l16 + (k & 3) * 1024, pso[s] + (k >> 2) * 4096, 16);
        };
        gemm4(0, cur);
        if (!F1_EARLY) {
            VMCNT(0);
            if (lane == 0) __hip_atomic_store((GAS u64*)(ws + WS_F1 + (wgid * 3 + t) * 128), tag, RLX_AGENT);
        }
        STAMP(6);
        {
            int ps = (lane & 3) + rotp; ps = ps >= 3 ? ps - 3 : ps;
            const int pw = b * 32 + xcd * 4 + ((lane & 3) == 3 ? hl : ((hl + 1 + ps) & 3));
            GAS u64* fp = (GAS u64*)(ws + WS_F1 + (pw * 3 + t) * 128);
            unsigned got = 0;
            auto poll = [&]() -> unsigned { const u64 f = __hip_atomic_load(fp, RLX_AGENT); const unsigned r = (unsigned)__builtin_amdgcn_ballot_w64(f == tag) & 7u; CBAR(); return r; };
            unsigned rdy = 0;
            for (unsigned sp = 0; !(rdy & 1u) && sp < SPIN_CAP; ++sp) rdy = poll();
            ld8(0);
            if (rdy & 2u) { ld8(1); got |= 2u; }
            if (rdy & 4u) { ld8(2); got |= 4u; }
            STAMP(11);
            if (got != 6u) {
                rdy = poll();
                if (!(got & 2u) && (rdy & 2u)) { ld8(1); got |= 2u; }
                if (!(got & 4u) && (rdy & 4u)) { ld8(2); got |= 4u; }
            }
            gemm4(4, pf[0]);
            if (!(got & 2u)) {
                for (unsigned sp = 0; !(rdy & 2u) && sp < SPIN_CAP; ++sp) rdy = poll();
                ld8(1);
                if (!(got & 4u) && (rdy & 4u)) { ld8(2); got |= 4u; }
            }
            STAMP(12);
            gemm4(8, pf[1]);
            if (!(got & 4u)) {
                for (unsigned sp = 0; !(rdy & 4u) && sp < SPIN_CAP; ++sp) rdy = poll();
                ld8(2);
            }
            STAMP(13);
            gemm4(12, pf[2]);
        }
        STAMP(7);
        {
            const __amdgpu_buffer_rsrc_t rp = mkrsrc(ws + WS_P + wgid * 24576 + t * 8192, 8192);
#pragma unroll
            for (int d = 0; d < 4; ++d) {
                __builtin_amdgcn_raw_buffer_store_b128(pack8(acc[d], 0), rp, l16 + (2 * d) * 1024, 0, 16);
                __builtin_amdgcn_raw_buffer_store_b128(pack8(acc[d], 1), rp, l16 + (2 * d + 1) * 1024, 0, 16);
            }
        }
        ctl[C_DONE / 4] = 1;
        VMCNT(0);
        if (lane < 2) __hip_atomic_store((GAS u64*)(ws + WS_F2 + ((wgid * 3 + t) * 2 + lane) * 128), tag, RLX_AGENT);
        STAMP(8);
    }
    if (xcd < 6 && w < 4) {
        const int t = xcd >> 1, f = 4 * (xcd & 1) + w;
        if (w == 3) {
            for (unsigned sp = 0; rfl(lds_ld(Lb + CTL + C_DONE)) == 0u && sp < SPIN_CAP; ++sp) __builtin_amdgcn_s_sleep(2);
            GAS u64* fp = (GAS u64*)(ws + WS_F2 + (((b * 32 + (lane & 7) * 4 + hl) * 3 + t) * 2 + (xcd & 1)) * 128);
            for (unsigned sp = 0; sp < SPIN_CAP; ++sp) {
                const u64 fl = __hip_atomic_load(fp, RLX_AGENT);
                if (__builtin_amdgcn_ballot_w64(fl != tag) == 0ull) break;
            }
            lds_st(Lb + CTL + C_READY, 1);
            if (xcd == 0 && hl == 0 && lane == 0) __hip_atomic_store((GAS u64*)(ws + WS_EPOCH + b * 256), tag, RLX_AGENT);
        } else {
            for (unsigned sp = 0; rfl(lds_ld(Lb + CTL + C_READY)) == 0u && sp < SPIN_CAP; ++sp) {}
        }
        CBAR();
        STAMP(9);
        v4u pp[8];
#pragma unroll
        for (int xx = 0; xx < 8; ++xx) pp[xx] = __builtin_amdgcn_raw_buffer_load_b128(rws, lane * 16, (int)WS_P + (b * 32 + xx * 4 + hl) * 24576 + t * 8192 + f * 1024, 16);
        float s[8];
#pragma unroll
        for (int k = 0; k < 8; ++k) s[k] = 0.f;
#pragma unroll
        for (int xx = 0; xx < 8; ++xx)
#pragma unroll
            for (int k = 0; k < 4; ++k) { s[2 * k] += __uint_as_float(pp[xx][k] << 16); s[2 * k + 1] += __uint_as_float(pp[xx][k] & 0xffff0000u); }
        v4u o = {cvtpk(s[0], s[1]), cvtpk(s[2], s[3]), cvtpk(s[4], s[5]), cvtpk(s[6], s[7])};
        *(v4u*)((char*)out + ((size_t)(b * 96 + 32 * t + (lane & 31)) * 512 + 128 * hl + 64 * (lane >> 5) + 8 * f) * 2) = o;
        STAMP(10);
    }
    STAMP_FLUSH(ws);
}

extern "C" void kernel_launch(void* const* d_in, const int* in_sizes, int n_in, void* d_out, int out_size, void* d_ws, size_t ws_size, hipStream_t stream) {
    (void)hipFuncSetAttribute((const void*)fused_kernel, hipFuncAttributeMaxDynamicSharedMemorySize, LDS_BYTES);
    fused_kernel<<<256, 320, LDS_BYTES, stream>>>(d_in[0], d_in[1], d_in[2], d_in[3], d_in[4], d_out, (char*)d_ws);
}
```

```cpp
#include <hip/hip_runtime.h>
#include <hip/hip_bf16.h>
#include <type_traits>

typedef unsigned v4u __attribute__((ext_vector_type(4)));
typedef unsigned v2u __attribute__((ext_vector_type(2)));
typedef float f32x16 __attribute__((ext_vector_type(16)));
typedef float f32x2 __attribute__((ext_vector_type(2)));
typedef __bf16 bf16x8 __attribute__((ext_vector_type(8)));
typedef __bf16 bf16x2 __attribute__((ext_vector_type(2)));
typedef short s16x4 __attribute__((ext_vector_type(4)));
typedef unsigned long long u64;
#define LAS __attribute__((address_space(3)))
#define GAS __attribute__((address_space(1)))
#define DI __device__ __forceinline__
#define RLX_AGENT __ATOMIC_RELAXED, __HIP_MEMORY_SCOPE_AGENT

#define KV_AUX 2
#define KV_D 5
#define W_D 3
#define CLR_SLEEP 4
#define LD_SLEEP 1
#define CP_SLEEP 1
#define KV_PRE 0
#define X_D 3
#define LD_PRIO 3
#define TROT idx
#define F1_EARLY 1
#define P_SLEEP 0
#define CODE_PF 1
#define XS1 1
#define XS2 3
#define XS3 9
#define TYPEB 0
#define WO_START 24
#define WO_D 5
#define KV_START 1

#define STAMP(i)
#define STAMP_DECL
#define STAMP_FLUSH(ws)

constexpr int SLOT = 8192;
constexpr int P_BASE = 0, X_BASE = 8 * SLOT, S_BASE = 11 * SLOT, CTL = 19 * SLOT;
constexpr int LDS_BYTES = CTL + 512;
constexpr int C_FULLK = 0, C_FULLW = 4, C_G = 16  , C_READY = 64, C_DONE = 68, C_RDY = 72;
constexpr size_t WS_EPOCH = 0;
constexpr size_t WS_F1 = 4096;
constexpr size_t WS_F2 = WS_F1 + 768 * 128;
constexpr size_t WS_O = WS_F2 + 1536 * 128;
constexpr size_t WS_P = WS_O + 256 * 24576;
constexpr int WS_BYTES = (int)(WS_P + 256 * 24576);
constexpr unsigned SPIN_CAP = 1u << 22;

DI f32x16 mfma32(v4u a, v4u b, f32x16 c) {
    return __builtin_amdgcn_mfma_f32_32x32x16_bf16(__builtin_bit_cast(bf16x8, a), __builtin_bit_cast(bf16x8, b), c, 0, 0, 0);
}
DI unsigned cvtpk(float lo, float hi) { f32x2 v = {lo, hi}; bf16x2 b = __builtin_convertvector(v, bf16x2); return __builtin_bit_cast(unsigned, b); }
DI __amdgpu_buffer_rsrc_t mkrsrc(const void* p, int bytes) { return __builtin_amdgcn_make_buffer_rsrc((void*)p, 0, bytes, 0x00020000); }
DI unsigned rfl(unsigned x) { return __builtin_amdgcn_readfirstlane(x); }
DI v4u pack8(const f32x16& a, int s) {
    return v4u{cvtpk(a[8 * s], a[8 * s + 1]), cvtpk(a[8 * s + 2], a[8 * s + 3]), cvtpk(a[8 * s + 4], a[8 * s + 5]), cvtpk(a[8 * s + 6], a[8 * s + 7])};
}
DI float swapmax(float x) { auto r = __builtin_amdgcn_permlane32_swap(__float_as_uint(x), __float_as_uint(x), false, false); return fmaxf(__uint_as_float(r[0]), __uint_as_float(r[1])); }
DI float swapsum(float x) { auto r = __builtin_amdgcn_permlane32_swap(__float_as_uint(x), __float_as_uint(x), false, false); return __uint_as_float(r[0]) + __uint_as_float(r[1]); }
DI float max3f(float a, float b, float c) { float r; asm("v_max3_f32 %0, %1, %2, %3" : "=v"(r) : "v"(a), "v"(b), "v"(c)); return r; }
DI float fadd_s(float a, float b) { float r; asm("v_add_f32_e32 %0, %1, %2" : "=v"(r) : "v"(a), "v"(b)); return r; }
#define SB() __builtin_amdgcn_sched_barrier(0)
#define OPAQUE(x) asm volatile("" : "+v"(x))
#define CBAR() asm volatile("" ::: "memory")
#define VMCNT(n) asm volatile("s_waitcnt vmcnt(%0)" :: "i"(n) : "memory")
#define LGKM0() asm volatile("s_waitcnt lgkmcnt(0)" ::: "memory")

DI unsigned lds_ld(unsigned addr) { unsigned r; asm volatile("ds_read_b32 %0, %1\n\ts_waitcnt lgkmcnt(0)" : "=v"(r) : "v"(addr) : "memory"); return r; }
DI void lds_st(unsigned addr, unsigned v) { asm volatile("ds_write_b32 %0, %1" :: "v"(addr), "v"(v) : "memory"); }
DI unsigned lds_min3(unsigned addr) {
    unsigned a, b, c;
    asm volatile("ds_read_b32 %0, %3\n\tds_read_b32 %1, %3 offset:4\n\tds_read_b32 %2, %3 offset:8\n\ts_waitcnt lgkmcnt(0)" : "=&v"(a), "=&v"(b), "=&v"(c) : "v"(addr) : "memory");
    return rfl(min(a, min(b, c)));
}

template <int AUX> DI void dma_chunk(__amdgpu_buffer_rsrc_t rs, LAS char* slot, const int (&voff)[4], int soff, int half) {
    __builtin_amdgcn_raw_ptr_buffer_load_lds(rs, (LAS void*)slot, 16, voff[0], soff, 0, AUX);
    __builtin_amdgcn_raw_ptr_buffer_load_lds(rs, (LAS void*)slot, 16, voff[1], soff, 1024, AUX);
    __builtin_amdgcn_raw_ptr_buffer_load_lds(rs, (LAS void*)slot, 16, voff[2], soff, 2048, AUX);
    __builtin_amdgcn_raw_ptr_buffer_load_lds(rs, (LAS void*)slot, 16, voff[3], soff, 3072, AUX);
    __builtin_amdgcn_raw_ptr_buffer_load_lds(rs, (LAS void*)(slot + 4096), 16, voff[0], soff + half, 0, AUX);
    __builtin_amdgcn_raw_ptr_buffer_load_lds(rs, (LAS void*)(slot + 4096), 16, voff[1], soff + half, 1024, AUX);
    __builtin_amdgcn_raw_ptr_buffer_load_lds(rs, (LAS void*)(slot + 4096), 16, voff[2], soff + half, 2048, AUX);
    __builtin_amdgcn_raw_ptr_buffer_load_lds(rs, (LAS void*)(slot + 4096), 16, voff[3], soff + half, 3072, AUX);
}
DI v2u tr_rd(unsigned addr, int off) { return __builtin_bit_cast(v2u, __builtin_amdgcn_ds_read_tr16_b64_v4i16((LAS s16x4*)(size_t)(addr + off))); }
DI v4u row_rd(unsigned addr) { return *(LAS v4u*)(size_t)addr; }
DI v4u tr_frag(unsigned tb, int off, int s, int ct) {
    v2u l2 = tr_rd(tb ^ (unsigned)(64 * ct), off + s * 4096), h2 = tr_rd(tb ^ (unsigned)(64 * ct + 16), off + s * 4096 + 1024);
    return v4u{l2[0], l2[1], h2[0], h2[1]};
}
DI void tr_frag_g(unsigned tb, int off, int g, v4u (&fr)[2][4]) { fr[g & 1][g >> 1] = tr_frag(tb, off, g & 1, g >> 1); }
DI void tr_chunk(unsigned tb, int off, v4u (&fr)[2][4]) {
#pragma unroll
    for (int g = 0; g < 8; ++g) tr_frag_g(tb, off, g, fr);
}
DI void tro_frag_g(unsigned tb, int off, int g, v4u (&fr)[2][4]) {
    const unsigned a = tb ^ (unsigned)(32 * (g >> 1));
    v2u l2 = tr_rd(a, off + (g & 1) * 4096), h2 = tr_rd(a, off + (g & 1) * 4096 + 1024);
    fr[g & 1][g >> 1] = v4u{l2[0], l2[1], h2[0], h2[1]};
}
template <bool NEXT> DI void gemm_step(f32x16 (&acc)[4], const v4u (&cur)[2][4], v4u b0, v4u b1, unsigned tb, int off, v4u (&nxt)[2][4]) {
#pragma unroll
    for (int g = 0; g < 8; ++g) {
        acc[g >> 1] = mfma32(cur[g & 1][g >> 1], (g & 1) ? b1 : b0, acc[g >> 1]);
        if (NEXT) tro_frag_g(tb, off, g, nxt);
        SB();
    }
}

__global__ __launch_bounds__(320, 2) void fused_kernel(const void* __restrict__ x, const void* __restrict__ K, const void* __restrict__ V,
                                                      const void* __restrict__ wq, const void* __restrict__ wo, void* __restrict__ out, char* ws) {
    extern __shared__ __attribute__((aligned(16))) char smem[];
    LAS char* L = (LAS char*)smem;
    const unsigned Lb = (unsigned)(size_t)L;
    const int tid = threadIdx.x, lane = tid & 63, w = rfl(tid >> 6);
    const int xcd = blockIdx.x & 7, idx = blockIdx.x >> 3;
    const int hl = idx & 3, b = idx >> 2, h = xcd * 4 + hl, bh = b * 32 + h;
    const int wgid = bh;
    STAMP_DECL
    STAMP(0);
    const __amdgpu_buffer_rsrc_t rws = mkrsrc(ws, WS_BYTES);
    const int cb0 = ((b >> 1) + hl) & 3, rotp = b % 3;
    u64 tag = 0;
    constexpr int npre = 0;
    const int pos = lane & 15, rq = lane >> 4;
    auto wq_off = [&](int n) -> int { return ((((n >> 2) + cb0) & 3) * 128 + ((n & 3) ^ (2 * (b & 1))) * 32) * 8192 + h * 256; };
    {
        unsigned long long pc; int len;
        asm volatile("s_getpc_b64 %0\n.Lfk_begin:\n\ts_mov_b32 %1, .Lfk_end-.Lfk_begin" : "=s"(pc), "=s"(len));
        __builtin_amdgcn_raw_ptr_buffer_load_lds(mkrsrc((const void*)pc, len), (LAS void*)(L + CTL + 256), 4, lane * 4, (idx * 5 + w) * 256, 0, 0);
    }
    {
        const bool isx = w < 3;
        const int sh = isx ? 10 : 13;
        const __amdgpu_buffer_rsrc_t r0 = mkrsrc(isx ? (const char*)x + (size_t)b * 98304 : (const char*)wq, isx ? 98304 : 512 * 8192);
        int vo[4];
#pragma unroll
        for (int k = 0; k < 4; ++k) vo[k] = ((4 * k + rq) << sh) + 16 * (pos ^ (rq << 2) ^ k) - 1024 * k;
        dma_chunk<0>(r0, L + (isx ? X_BASE + w * SLOT : P_BASE + (w - 3) * SLOT), vo, isx ? w * 32768 + cb0 * 256 : wq_off(w - 3), 16 << sh);
    }

    if (w >= 3) {
        int vo1k[4];
#pragma unroll
        for (int k = 0; k < 4; ++k) vo1k[k] = (4 * k + rq) * 1024 + 16 * (pos ^ (rq << 2) ^ k) - 1024 * k;
        const __amdgpu_buffer_rsrc_t rwo = mkrsrc(wo, 4096 * 1024);
        auto wo_off = [&](int ci) -> int {
            int head = hl;
            if (ci >= 4) { int ps = (ci >> 2) - 1 + rotp; ps = ps >= 3 ? ps - 3 : ps; head = (hl + 1 + ps) & 3; }
            return ((xcd * 4 + head) * 128 + (ci & 3) * 32) * 1024 + hl * 256;
        };
        __builtin_amdgcn_s_setprio(LD_PRIO);
        unsigned prog = 0;
        unsigned fulla = Lb + CTL + (w == 3 ? C_FULLK : C_FULLW);
        OPAQUE(fulla);
        auto gate = [&](unsigned need) {
            if (__builtin_expect(prog < need, 0)) {
                for (unsigned sp = 0; prog < need && sp < SPIN_CAP; ++sp) { if (sp) __builtin_amdgcn_s_sleep(LD_SLEEP); prog = lds_min3(Lb + CTL + C_G); }
            }
        };
        if (w == 3) {
            const __amdgpu_buffer_rsrc_t rx = mkrsrc((const char*)x + (size_t)b * 98304, 98304);
            const __amdgpu_buffer_rsrc_t rk = mkrsrc((const char*)K + (size_t)bh * 131072, 131072);
            const __amdgpu_buffer_rsrc_t rv = mkrsrc((const char*)V + (size_t)bh * 131072, 131072);
            int vo256[4];
#pragma unroll
            for (int k = 0; k < 4; ++k) vo256[k] = rq * 256 + 16 * (pos ^ (rq << 2) ^ k);
            const int trot = TROT;
            tag = __hip_atomic_load((GAS u64*)(ws + WS_EPOCH + b * 256), RLX_AGENT) + 1;
#pragma unroll
            for (int i = 3; i < 12; ++i) {
                const int cbi = i / 3, r = i % 3;
                if (i >= 3 + X_D) { VMCNT(8 * (X_D - 1)); lds_st(fulla, npre + i - X_D + 1); }
                if (i == 9) gate(1);
                dma_chunk<0>(rx, L + (cbi == 1 ? S_BASE + 2 * SLOT : (cbi == 2 ? S_BASE + 5 * SLOT : X_BASE)) + r * SLOT, vo1k, r * 32768 + ((cbi + cb0) & 3) * 256, 16384);
                if (i == 3 + X_D - 1) { VMCNT(8 * X_D); __builtin_amdgcn_s_barrier(); OPAQUE(tag); }
            }
            unsigned pubd = npre + 12 - X_D;
            prog = lds_min3(Lb + CTL + C_G);
            if (prog < KV_START) {
#pragma unroll
                for (int d = X_D - 1; d >= 0; --d) { VMCNT(8 * d); lds_st(fulla, npre + 12 - d); }
                pubd = npre + 12;
                gate(KV_START);
            }
            STAMP(1);
#pragma unroll 1
            for (int p = npre >> 1; p < 16; ++p) {
                const int c = 2 * p;
                LAS char* sl = L + ((c & 8) ? S_BASE : P_BASE) + (c & 7) * SLOT;
                VMCNT(8 * (KV_D - 1)); lds_st(fulla, max(pubd, (unsigned)(12 + c - KV_D + 1)));
                gate(c + 1);
                if (p > 0) dma_chunk<KV_AUX>(rv, sl, vo256, ((p - 1 + trot) & 15) * 8192, 4096);
                else dma_chunk<KV_AUX>(rk, sl, vo256, (trot & 15) * 8192, 4096);
                VMCNT(8 * (KV_D - 1)); lds_st(fulla, max(pubd, (unsigned)(12 + c + 1 - KV_D + 1)));
                gate(c + 2);
                if (p < 15) dma_chunk<KV_AUX>(rk, sl + SLOT, vo256, ((p + 1 + trot) & 15) * 8192, 4096);
                else dma_chunk<KV_AUX>(rv, sl + SLOT, vo256, ((15 + trot) & 15) * 8192, 4096);
            }
            STAMP(2);
#pragma unroll
            for (int d = KV_D - 1; d >= 0; --d) { VMCNT(8 * d); lds_st(fulla, 12 + 32 - d); }
            STAMP(3);
            {
                int ps = (lane & 3) + rotp; ps = ps >= 3 ? ps - 3 : ps;
                const int pw = b * 32 + xcd * 4 + ((hl + 1 + ps) & 3);
                GAS u64* fp = (GAS u64*)(ws + WS_F1 + (pw * 3 + min((lane >> 2) & 3, 2)) * 128);
                unsigned m = 0;
                for (unsigned sp = 0; m != 0x777u; ++sp) {
                    const u64 f = __hip_atomic_load(fp, RLX_AGENT);
                    unsigned r = (unsigned)__builtin_amdgcn_ballot_w64(f == tag) & 0x777u;
                    if (sp >= SPIN_CAP) r = 0x777u;
                    if (r != m) { m = r; lds_st(Lb + CTL + C_RDY, m); }
                }
            }
            STAMP(4);
        } else {
            const __amdgpu_buffer_rsrc_t rwq = mkrsrc(wq, 512 * 8192);
            int vo8k[4];
#pragma unroll
            for (int k = 0; k < 4; ++k) vo8k[k] = (4 * k + rq) * 8192 + 16 * (pos ^ (rq << 2) ^ k) - 1024 * k;
#pragma unroll
            for (int n = 2; n < 16; ++n) {
                if (n >= 2 + W_D) { VMCNT(8 * (W_D - 1)); lds_st(fulla, n - W_D + 1); }
                if (n == 10) gate(XS1 + 2);
                if (n == 13) gate(XS2 + 2);
                dma_chunk<0>(rwq, L + (n & 8 ? S_BASE : P_BASE) + (n & 7) * SLOT, vo8k, wq_off(n), 131072);
                if (n == 3) { VMCNT(16); __builtin_amdgcn_s_barrier(); }
            }
            STAMP(1);
#pragma unroll
            for (int d = W_D - 1; d >= 0; --d) { VMCNT(8 * d); lds_st(fulla, 16 - d); }
            int vow[4];
#pragma unroll
            for (int k = 0; k < 4; ++k) vow[k] = (4 * k + rq) * 1024 + 16 * (pos ^ (rq << 1)) - 1024 * k;
#pragma unroll 1
            for (int i = 0; i < 16; ++i) {
                const int ci = i < 3 ? 8 + i : (i < 11 ? i - 3 : i);
                gate(i < 3 ? WO_START : 30 + i);
                dma_chunk<0>(rwo, L + ci * SLOT, vow, wo_off(ci), 16384);
                VMCNT(8 * (WO_D - 1)); if (i + 2 > WO_D) lds_st(fulla, 16 + i + 2 - WO_D);
            }
#pragma unroll
            for (int d = WO_D - 2; d >= 0; --d) { VMCNT(8 * d); lds_st(fulla, 32 - d); }
            STAMP(2);
            STAMP(3);
        }
    } else {
        const int t = w;
        if (tid < 64) lds_st(Lb + CTL + tid * 4, 0u);
        tag = __hip_atomic_load((GAS u64*)(ws + WS_EPOCH + b * 256), RLX_AGENT) + 1;
        const int r32 = lane & 31, hh = lane >> 5;
        volatile LAS unsigned* ctl = (volatile LAS unsigned*)(L + CTL);
        const int fr_ = ((r32 & 3) << 2) | ((r32 >> 2) & 3);
        unsigned rbx = Lb + t * SLOT + ((256 * r32 + 16 * (hh ^ fr_)) ^ (128 * (b & 1)));
        const int pr = (r32 & 19) | ((r32 & 4) << 1) | ((r32 & 8) >> 1);
        const int fp_ = ((pr & 3) << 2) | ((pr >> 2) & 3);
        unsigned rbk = Lb + P_BASE + 256 * pr + 16 * (hh ^ fp_);
        const int blk = (lane >> 4) & 1, q4 = (lane & 15) >> 2, p4 = lane & 3;
        unsigned tb0 = Lb + 256 * (8 * hh + q4) + 64 * q4 + 16 * ((2 * blk + (p4 & 1)) ^ (2 * hh)) + 8 * (p4 >> 1);
        unsigned availW = 2, availK = 0;
        auto waitW = [&](unsigned need) { if (__builtin_expect(availW < need, 0)) for (unsigned sp = 0; availW < need && sp < SPIN_CAP; ++sp) { if (sp) __builtin_amdgcn_s_sleep(CP_SLEEP); availW = rfl(ctl[C_FULLW / 4]); } CBAR(); };
        auto waitKs = [&](unsigned need) { if (__builtin_expect(availK < need, 0)) for (unsigned sp = 0; availK < need && sp < SPIN_CAP; ++sp) { if (sp) __builtin_amdgcn_s_sleep(CP_SLEEP); availK = rfl(ctl[C_FULLK / 4]); } CBAR(); };
        auto waitK = [&](unsigned n) { waitKs(12 + n); };
        f32x16 acc[4];
#pragma unroll
        for (int d = 0; d < 4; ++d) acc[d] = f32x16{};
        {
            v4u fa[2][4], fb[2][4], xf[32];
            const unsigned tbw0 = tb0 + P_BASE, tbw1 = tb0 + S_BASE;
            asm volatile("s_waitcnt vmcnt(0) lgkmcnt(0)" ::: "memory");
            __builtin_amdgcn_s_barrier();
            CBAR();
            STAMP(14);
            xf[0] = row_rd(rbx + X_BASE); xf[1] = row_rd((rbx + X_BASE) ^ 32u);
            tr_chunk(tbw0, 0, fa);
#pragma unroll
            for (int ks = 2; ks < 8; ++ks) xf[ks] = row_rd(((rbx + X_BASE) ^ (unsigned)(32 * ks)));
            ctl[C_G / 4 + t] = 1;
#pragma unroll
            for (int n = 0; n < 16; ++n) {
                const int xq = n == XS1 ? 1 : (n == XS2 ? 2 : (n == XS3 ? 3 : 0));
                if (n & 1) waitW(n + 3 < 16 ? n + 3 : 16);
                if (xq) waitKs(npre + 3 * xq + t + 1);
                const int offn = ((n + 1) & 7) * SLOT;
                const unsigned tbw = n + 1 < 8 ? tbw0 : tbw1;
                const unsigned xn = rbx + (xq == 1 ? S_BASE + 2 * SLOT : (xq == 2 ? S_BASE + 5 * SLOT : X_BASE));
                v2u fw;
                SB();
#pragma unroll
                for (int g = 0; g < 8; ++g) {
                    if (n & 1) { acc[g >> 1] = mfma32(fb[g & 1][g >> 1], xf[2 * n + (g & 1)], acc[g >> 1]); if (n < 15) tr_frag_g(tbw, offn, g, fa); }
                    else { acc[g >> 1] = mfma32(fa[g & 1][g >> 1], xf[2 * n + (g & 1)], acc[g >> 1]); tr_frag_g(tbw, offn, g, fb); }
                    if (xq) xf[8 * xq + g] = row_rd(xn ^ (unsigned)(32 * g));
                    if (g == 3 && !(n & 1)) fw = *(volatile LAS v2u*)(L + CTL);
                    if (g == 0 && !(n & 1) && n > 0) ctl[C_G / 4 + t] = n + 1;
                    if (g == 7 && n == 14) ctl[C_G / 4 + t] = 16;
                    if (g == 7 && !(n & 1)) { availK = rfl(fw[0]); availW = max(2u, rfl(fw[1])); }
                    SB();
                }
            }
        }
        asm volatile("" :: "v"(tag));
        STAMP(1);
        constexpr float CS = 0.08838834764831845f * 1.4426950408889634f;
        constexpr float THR = 8.0f;
        v4u qf[8];
#pragma unroll
        for (int d = 0; d < 4; ++d) { acc[d] *= CS; qf[2 * d] = pack8(acc[d], 0); qf[2 * d + 1] = pack8(acc[d], 1); }
#pragma unroll
        for (int d = 0; d < 4; ++d) acc[d] = f32x16{};
        float lsum = 0.f;
        f32x16 sA, sB, negm; v4u kf[8], vf[2][4];
        unsigned tbk = tb0 + P_BASE; OPAQUE(tbk);
        unsigned ctla = Lb + CTL + C_FULLK, pka = Lb + CTL + C_G + 4 * t, relv = 18;
        OPAQUE(ctla); OPAQUE(pka); OPAQUE(relv);
        auto decide = [&](float tm, f32x16& nxt) {
            if (__builtin_expect(__builtin_amdgcn_ballot_w64(tm > THR) != 0ull, 0)) {
                const float dl = fmaxf(swapmax(tm), 0.f), f = __builtin_amdgcn_exp2f(-dl);
                lsum *= f;
#pragma unroll
                for (int d = 0; d < 4; ++d) acc[d] *= f;
#pragma unroll
                for (int i = 0; i < 16; ++i) { nxt[i] -= dl; negm[i] -= dl; }
            }
        };
        auto block = [&](f32x16& cur, f32x16& nxt, int vslot, auto KR) -> float {
            v4u p[2];
            float m = 0.f, m1 = 0.f, s0 = 0.f, s1 = 0.f; unsigned fk = 0;
            SB();
#pragma unroll
            for (int g = 0; g < 8; ++g) {
                nxt = mfma32(kf[g], qf[g], g == 0 ? negm : nxt);
                tr_frag_g(tbk, vslot * SLOT, g, vf);
                const int e0 = (3 * g + 1) / 2, e1 = (3 * (g + 1) + 1) / 2;
#pragma unroll
                for (int e = e0; e < e1; ++e) cur[e] = __builtin_amdgcn_exp2f(cur[e]);
                if (g >= 1) {
                    const int pp0 = ((3 * (g - 1) + 1) / 2) / 2, pp1 = e0 / 2;
#pragma unroll
                    for (int q = pp0; q < pp1; ++q) p[q >> 2][q & 3] = cvtpk(cur[2 * q], cur[2 * q + 1]);
                }
                SB();
            }
#pragma unroll
            for (int g = 0; g < 8; ++g) {
                acc[g & 3] = mfma32(vf[g >> 2][g & 3], p[g >> 2], acc[g & 3]);
                if (KR && g < 4) { kf[2 * g] = row_rd((rbk ^ (unsigned)(64 * g)) + (vslot + 1) * SLOT); kf[2 * g + 1] = row_rd((rbk ^ (unsigned)(64 * g + 32)) + (vslot + 1) * SLOT); }
                if (g == 0) { cur[12] = __builtin_amdgcn_exp2f(cur[12]); cur[13] = __builtin_amdgcn_exp2f(cur[13]); p[1][1] = cvtpk(cur[10], cur[11]); }
                if (g == 1) { cur[14] = __builtin_amdgcn_exp2f(cur[14]); cur[15] = __builtin_amdgcn_exp2f(cur[15]); p[1][2] = cvtpk(cur[12], cur[13]); }
                if (g == 2) { p[1][3] = cvtpk(cur[14], cur[15]); m = max3f(nxt[0], nxt[1], nxt[2]); m1 = max3f(nxt[3], nxt[4], nxt[5]); }
                if (g == 3) { m = max3f(m, nxt[6], nxt[7]); m1 = max3f(m1, nxt[8], nxt[9]); }
                if (g == 4) { m = max3f(m, nxt[10], nxt[11]); m1 = max3f(m1, nxt[12], nxt[13]); relv += KR ? 2 : 1; *(volatile LAS unsigned*)(size_t)pka = relv; fk = *(volatile LAS unsigned*)(size_t)ctla; }
                if (g == 5) { m = max3f(m, nxt[14], nxt[15]); m = max3f(m, m1, m1); }
                if (g >= 4) {
#pragma unroll
                    for (int e = 4 * (g - 4); e < 4 * (g - 4) + 4; e += 2) { s0 = fadd_s(s0, cur[e]); s1 = fadd_s(s1, cur[e + 1]); }
                }
                SB();
            }
            lsum += s0 + s1;
            OPAQUE(lsum);
            availK = rfl(fk);
            return m;
        };
        waitK(1);
#pragma unroll
        for (int ks = 0; ks < 8; ++ks) kf[ks] = row_rd(rbk ^ (unsigned)(32 * ks));
#pragma unroll
        for (int ks = 0; ks < 8; ++ks) sA = mfma32(kf[ks], qf[ks], ks == 0 ? f32x16{} : sA);
        waitK(2);
#pragma unroll
        for (int ks = 0; ks < 8; ++ks) kf[ks] = row_rd((rbk ^ (unsigned)(32 * ks)) + SLOT);
        *(volatile LAS unsigned*)(size_t)pka = relv;
        {
            float m = fmaxf(sA[0], sA[1]);
#pragma unroll
            for (int i = 2; i < 16; i += 2) m = fmaxf(fmaxf(m, sA[i]), sA[i + 1]);
            m = swapmax(m);
#pragma unroll
            for (int i = 0; i < 16; ++i) { sA[i] -= m; negm[i] = -m; }
            OPAQUE(negm);
        }
        STAMP(2);
        {
            constexpr std::integral_constant<bool, true> T{};
            constexpr std::integral_constant<bool, false> F{};
#pragma unroll 1
            for (int j = 0; j < 12; j += 4) {
                waitK(2 * j + 4); decide(block(sA, sB, 2, T), sB);
                waitK(2 * j + 6); decide(block(sB, sA, 4, T), sA);
                waitK(2 * j + 8); decide(block(sA, sB, 6, T), sB);
                tbk ^= S_BASE; rbk ^= S_BASE;
                waitK(2 * j + 10); decide(block(sB, sA, 0, T), sA);
            }
            waitK(28); decide(block(sA, sB, 2, T), sB);
            waitK(30); decide(block(sB, sA, 4, T), sA);
            waitK(31);
            decide(block(sA, sB, 6, F), sB);
            STAMP(3);
        }
        const int l16 = lane * 16;
        v4u pf[3][8];
        unsigned got = 0;
        auto rdy = [&]() -> unsigned { const unsigned r = (rfl(ctl[C_RDY / 4]) >> (4 * t)) & 7u & ~got; CBAR(); return r; };
        auto take = [&](int k) {
            got |= 1u << k;
            int ps = k + rotp; ps = ps >= 3 ? ps - 3 : ps;
            const int so = (int)WS_O + (b * 32 + xcd * 4 + ((hl + 1 + ps) & 3)) * 24576 + t * 8192;
#pragma unroll
            for (int i = 0; i < 8; ++i) pf[k][i] = __builtin_amdgcn_raw_buffer_load_b128(rws, l16 + (i & 3) * 1024, so + (i >> 2) * 4096, 16);
        };
        auto must = [&](int k) {
            unsigned r = rdy();
            if (!(got >> k & 1)) { for (unsigned sp = 0; !(r >> k & 1) && sp < 4 * SPIN_CAP; ++sp) r = rdy(); take(k); }
#pragma unroll
            for (int j = k + 1; j < 3; ++j) if (r >> j & 1) take(j);
        };
        { const unsigned r = rdy(); if (r & 1) take(0); if (r & 2) take(1); }
        v4u p15[2];
        {
            float s0 = 0.f, s1 = 0.f;
#pragma unroll
            for (int g = 0; g < 8; ++g) {
                const float e0 = __builtin_amdgcn_exp2f(sB[2 * g]), e1 = __builtin_amdgcn_exp2f(sB[2 * g + 1]);
                s0 += e0; s1 += e1; p15[g >> 2][g & 3] = cvtpk(e0, e1);
            }
            lsum += s0 + s1;
        }
        float inv = 1.0f / swapsum(lsum);
        OPAQUE(inv); OPAQUE(p15[0]); OPAQUE(p15[1]);
        waitW(20);
        waitK(32);
        tr_chunk(tbk, 7 * SLOT, vf);
        unsigned tbo = Lb + 256 * (8 * hh + q4) + 16 * ((8 * (p4 & 1) + blk) ^ (q4 << 1)) + 8 * (p4 >> 1);
        OPAQUE(tbo);
        v4u fa[2][4], fb[2][4];
        SB();
#pragma unroll
        for (int g = 0; g < 8; ++g) {
            tro_frag_g(tbo, 0, g, fa);
            acc[g >> 1] = mfma32(vf[g & 1][g >> 1], p15[g & 1], acc[g >> 1]);
            SB();
        }
        STAMP(4);
        v4u cur[8];
        {
            const __amdgpu_buffer_rsrc_t ro = mkrsrc(ws + WS_O + wgid * 24576 + t * 8192, 8192);
#pragma unroll
            for (int d = 0; d < 4; ++d) {
                acc[d] *= inv; cur[2 * d] = pack8(acc[d], 0); cur[2 * d + 1] = pack8(acc[d], 1);
                __builtin_amdgcn_raw_buffer_store_b128(cur[2 * d], ro, l16 + (2 * d) * 1024, 0, 16);
                __builtin_amdgcn_raw_buffer_store_b128(cur[2 * d + 1], ro, l16 + (2 * d + 1) * 1024, 0, 16);
            }
        }
#pragma unroll
        for (int d = 0; d < 4; ++d) acc[d] = f32x16{};
        STAMP(5);
        auto gemm4 = [&](unsigned tb, const v4u* cur, auto FIRST, auto LAST) {
            OPAQUE(tb);
            SB();
            gemm_step<true>(acc, fa, cur[0], cur[1], tb, SLOT, fb);
            gemm_step<true>(acc, fb, cur[2], cur[3], tb, 2 * SLOT, fa);
            if (FIRST) {
                VMCNT(0);
                if (lane == 0) __hip_atomic_store((GAS u64*)(ws + WS_F1 + (wgid * 3 + t) * 128), tag, RLX_AGENT);
                SB();
            }
            gemm_step<true>(acc, fa, cur[4], cur[5], tb, 3 * SLOT, fb);
            if (!LAST) gemm_step<true>(acc, fb, cur[6], cur[7], tb, 4 * SLOT, fa);
            else gemm_step<false>(acc, fb, cur[6], cur[7], 0, 0, fa);
        };
        {
            constexpr std::integral_constant<bool, true> T{};
            constexpr std::integral_constant<bool, false> F{};
            waitW(24);
            gemm4(tbo, cur, T, F);
            STAMP(6);
            must(0); waitW(27);
            STAMP(11);
            gemm4(tbo + 4 * SLOT, pf[0], F, F);
            must(1); waitW(29);
            STAMP(12);
            gemm4(tbo + 8 * SLOT, pf[1], F, F);
            must(2); waitW(32);
            STAMP(13);
            gemm4(tbo + 12 * SLOT, pf[2], F, T);
        }
        STAMP(7);
        {
            const __amdgpu_buffer_rsrc_t rp = mkrsrc(ws + WS_P + wgid * 24576 + t * 8192, 8192);
#pragma unroll
            for (int d = 0; d < 4; ++d) {
                __builtin_amdgcn_raw_buffer_store_b128(pack8(acc[d], 0), rp, l16 + (2 * d) * 1024, 0, 16);
                __builtin_amdgcn_raw_buffer_store_b128(pack8(acc[d], 1), rp, l16 + (2 * d + 1) * 1024, 0, 16);
            }
        }
        ctl[C_DONE / 4] = 1;
        VMCNT(0);
        if (lane < 2) __hip_atomic_store((GAS u64*)(ws + WS_F2 + ((wgid * 3 + t) * 2 + lane) * 128), tag, RLX_AGENT);
        STAMP(8);
    }
    if (xcd < 6 && w < 4) {
        const int t = xcd >> 1, f = 4 * (xcd & 1) + w;
        if (w == 3) {
            for (unsigned sp = 0; rfl(lds_ld(Lb + CTL + C_DONE)) == 0u && sp < SPIN_CAP; ++sp) __builtin_amdgcn_s_sleep(2);
            GAS u64* fp = (GAS u64*)(ws + WS_F2 + (((b * 32 + (lane & 7) * 4 + hl) * 3 + t) * 2 + (xcd & 1)) * 128);
            for (unsigned sp = 0; sp < SPIN_CAP; ++sp) {
                const u64 fl = __hip_atomic_load(fp, RLX_AGENT);
                if (__builtin_amdgcn_ballot_w64(fl != tag) == 0ull) break;
            }
            lds_st(Lb + CTL + C_READY, 1);
            if (xcd == 0 && hl == 0 && lane == 0) __hip_atomic_store((GAS u64*)(ws + WS_EPOCH + b * 256), tag, RLX_AGENT);
        } else {
            for (unsigned sp = 0; rfl(lds_ld(Lb + CTL + C_READY)) == 0u && sp < SPIN_CAP; ++sp) {}
        }
        CBAR();
        STAMP(9);
        v4u pp[8];
#pragma unroll
        for (int xx = 0; xx < 8; ++xx) pp[xx] = __builtin_amdgcn_raw_buffer_load_b128(rws, lane * 16, (int)WS_P + (b * 32 + xx * 4 + hl) * 24576 + t * 8192 + f * 1024, 16);
        float s[8];
#pragma unroll
        for (int k = 0; k < 8; ++k) s[k] = 0.f;
#pragma unroll
        for (int xx = 0; xx < 8; ++xx)
#pragma unroll
            for (int k = 0; k < 4; ++k) { s[2 * k] += __uint_as_float(pp[xx][k] << 16); s[2 * k + 1] += __uint_as_float(pp[xx][k] & 0xffff0000u); }
        v4u o = {cvtpk(s[0], s[1]), cvtpk(s[2], s[3]), cvtpk(s[4], s[5]), cvtpk(s[6], s[7])};
        *(v4u*)((char*)out + ((size_t)(b * 96 + 32 * t + (lane & 31)) * 512 + 128 * hl + 64 * (lane >> 5) + 8 * f) * 2) = o;
        STAMP(10);
    }
    STAMP_FLUSH(ws);
    asm volatile(".Lfk_end:");
}

extern "C" void kernel_launch(void* const* d_in, const int* in_sizes, int n_in, void* d_out, int out_size, void* d_ws, size_t ws_size, hipStream_t stream) {
    (void)hipFuncSetAttribute((const void*)fused_kernel, hipFuncAttributeMaxDynamicSharedMemorySize, LDS_BYTES);
    fused_kernel<<<256, 320, LDS_BYTES, stream>>>(d_in[0], d_in[1], d_in[2], d_in[3], d_in[4], d_out, (char*)d_ws);
}
```

```cpp
#include <hip/hip_runtime.h>
#include <hip/hip_bf16.h>
#include <type_traits>

typedef unsigned v4u __attribute__((ext_vector_type(4)));
typedef unsigned v2u __attribute__((ext_vector_type(2)));
typedef float f32x16 __attribute__((ext_vector_type(16)));
typedef float f32x2 __attribute__((ext_vector_type(2)));
typedef __bf16 bf16x8 __attribute__((ext_vector_type(8)));
typedef __bf16 bf16x2 __attribute__((ext_vector_type(2)));
typedef short s16x4 __attribute__((ext_vector_type(4)));
typedef unsigned long long u64;
#define LAS __attribute__((address_space(3)))
#define GAS __attribute__((address_space(1)))
#define DI __device__ __forceinline__
#define RLX_AGENT __ATOMIC_RELAXED, __HIP_MEMORY_SCOPE_AGENT

#define KV_AUX 2
#define KV_D 5
#define W_D 3
#define CLR_SLEEP 4
#define LD_SLEEP 1
#define CP_SLEEP 1
#define KV_PRE 0
#define X_D 3
#define LD_PRIO 3
#define TROT idx
#define F1_EARLY 1
#define P_SLEEP 0
#define CODE_PF 1
#define F1_POS 2
#define XS1 1
#define XS2 3
#define XS3 9
#define TYPEB 0
#define WO_START 24
#define WO_D 5
#define KV_START 1

#define STAMP(i)
#define STAMP_DECL
#define STAMP_FLUSH(ws)

constexpr int SLOT = 8192;
constexpr int P_BASE = 0, X_BASE = 8 * SLOT, S_BASE = 11 * SLOT, CTL = 19 * SLOT;
constexpr int LDS_BYTES = CTL + 512;
constexpr int C_FULLK = 0, C_FULLW = 4, C_G = 16  , C_READY = 64, C_DONE = 68, C_RDY = 8;
constexpr size_t WS_EPOCH = 0;
constexpr size_t WS_F1 = 4096;
constexpr size_t WS_F2 = WS_F1 + 768 * 128;
constexpr size_t WS_O = WS_F2 + 1536 * 128;
constexpr size_t WS_P = WS_O + 256 * 24576;
constexpr int WS_BYTES = (int)(WS_P + 256 * 24576);
constexpr unsigned SPIN_CAP = 1u << 22;

DI f32x16 mfma32(v4u a, v4u b, f32x16 c) {
    return __builtin_amdgcn_mfma_f32_32x32x16_bf16(__builtin_bit_cast(bf16x8, a), __builtin_bit_cast(bf16x8, b), c, 0, 0, 0);
}
DI unsigned cvtpk(float lo, float hi) { f32x2 v = {lo, hi}; bf16x2 b = __builtin_convertvector(v, bf16x2); return __builtin_bit_cast(unsigned, b); }
DI __amdgpu_buffer_rsrc_t mkrsrc(const void* p, int bytes) { return __builtin_amdgcn_make_buffer_rsrc((void*)p, 0, bytes, 0x00020000); }
DI unsigned rfl(unsigned x) { return __builtin_amdgcn_readfirstlane(x); }
DI v4u pack8(const f32x16& a, int s) {
    return v4u{cvtpk(a[8 * s], a[8 * s + 1]), cvtpk(a[8 * s + 2], a[8 * s + 3]), cvtpk(a[8 * s + 4], a[8 * s + 5]), cvtpk(a[8 * s + 6], a[8 * s + 7])};
}
DI float swapmax(float x) { auto r = __builtin_amdgcn_permlane32_swap(__float_as_uint(x), __float_as_uint(x), false, false); return fmaxf(__uint_as_float(r[0]), __uint_as_float(r[1])); }
DI float swapsum(float x) { auto r = __builtin_amdgcn_permlane32_swap(__float_as_uint(x), __float_as_uint(x), false, false); return __uint_as_float(r[0]) + __uint_as_float(r[1]); }
DI float max3f(float a, float b, float c) { float r; asm("v_max3_f32 %0, %1, %2, %3" : "=v"(r) : "v"(a), "v"(b), "v"(c)); return r; }
DI float fadd_s(float a, float b) { float r; asm("v_add_f32_e32 %0, %1, %2" : "=v"(r) : "v"(a), "v"(b)); return r; }
#define SB() __builtin_amdgcn_sched_barrier(0)
#define OPAQUE(x) asm volatile("" : "+v"(x))
#define CBAR() asm volatile("" ::: "memory")
#define VMCNT(n) asm volatile("s_waitcnt vmcnt(%0)" :: "i"(n) : "memory")
#define LGKM0() asm volatile("s_waitcnt lgkmcnt(0)" ::: "memory")

DI unsigned lds_ld(unsigned addr) { unsigned r; asm volatile("ds_read_b32 %0, %1\n\ts_waitcnt lgkmcnt(0)" : "=v"(r) : "v"(addr) : "memory"); return r; }
DI void lds_st(unsigned addr, unsigned v) { asm volatile("ds_write_b32 %0, %1" :: "v"(addr), "v"(v) : "memory"); }
DI unsigned lds_min3(unsigned addr) {
    unsigned a, b, c;
    asm volatile("ds_read_b32 %0, %3\n\tds_read_b32 %1, %3 offset:4\n\tds_read_b32 %2, %3 offset:8\n\ts_waitcnt lgkmcnt(0)" : "=&v"(a), "=&v"(b), "=&v"(c) : "v"(addr) : "memory");
    return rfl(min(a, min(b, c)));
}

template <int AUX> DI void dma_chunk(__amdgpu_buffer_rsrc_t rs, LAS char* slot, const int (&voff)[4], int soff, int half) {
    __builtin_amdgcn_raw_ptr_buffer_load_lds(rs, (LAS void*)slot, 16, voff[0], soff, 0, AUX);
    __builtin_amdgcn_raw_ptr_buffer_load_lds(rs, (LAS void*)slot, 16, voff[1], soff, 1024, AUX);
    __builtin_amdgcn_raw_ptr_buffer_load_lds(rs, (LAS void*)slot, 16, voff[2], soff, 2048, AUX);
    __builtin_amdgcn_raw_ptr_buffer_load_lds(rs, (LAS void*)slot, 16, voff[3], soff, 3072, AUX);
    __builtin_amdgcn_raw_ptr_buffer_load_lds(rs, (LAS void*)(slot + 4096), 16, voff[0], soff + half, 0, AUX);
    __builtin_amdgcn_raw_ptr_buffer_load_lds(rs, (LAS void*)(slot + 4096), 16, voff[1], soff + half, 1024, AUX);
    __builtin_amdgcn_raw_ptr_buffer_load_lds(rs, (LAS void*)(slot + 4096), 16, voff[2], soff + half, 2048, AUX);
    __builtin_amdgcn_raw_ptr_buffer_load_lds(rs, (LAS void*)(slot + 4096), 16, voff[3], soff + half, 3072, AUX);
}
DI v2u tr_rd(unsigned addr, int off) { return __builtin_bit_cast(v2u, __builtin_amdgcn_ds_read_tr16_b64_v4i16((LAS s16x4*)(size_t)(addr + off))); }
DI v4u row_rd(unsigned addr) { return *(LAS v4u*)(size_t)addr; }
DI v4u tr_frag(unsigned tb, int off, int s, int ct) {
    v2u l2 = tr_rd(tb ^ (unsigned)(64 * ct), off + s * 4096), h2 = tr_rd(tb ^ (unsigned)(64 * ct + 16), off + s * 4096 + 1024);
    return v4u{l2[0], l2[1], h2[0], h2[1]};
}
DI void tr_frag_g(unsigned tb, int off, int g, v4u (&fr)[2][4]) { fr[g & 1][g >> 1] = tr_frag(tb, off, g & 1, g >> 1); }
DI void tr_chunk(unsigned tb, int off, v4u (&fr)[2][4]) {
#pragma unroll
    for (int g = 0; g < 8; ++g) tr_frag_g(tb, off, g, fr);
}
DI void tro_frag_g(unsigned tb, int off, int g, v4u (&fr)[2][4]) {
    const unsigned a = tb ^ (unsigned)(32 * (g >> 1));
    v2u l2 = tr_rd(a, off + (g & 1) * 4096), h2 = tr_rd(a, off + (g & 1) * 4096 + 1024);
    fr[g & 1][g >> 1] = v4u{l2[0], l2[1], h2[0], h2[1]};
}
template <bool NEXT> DI void gemm_step(f32x16 (&acc)[4], const v4u (&cur)[2][4], v4u b0, v4u b1, unsigned tb, int off, v4u (&nxt)[2][4]) {
#pragma unroll
    for (int g = 0; g < 8; ++g) {
        acc[g >> 1] = mfma32(cur[g & 1][g >> 1], (g & 1) ? b1 : b0, acc[g >> 1]);
        if (NEXT) tro_frag_g(tb, off, g, nxt);
        SB();
    }
}

__global__ __launch_bounds__(320, 2) void fused_kernel(const void* __restrict__ x, const void* __restrict__ K, const void* __restrict__ V,
                                                      const void* __restrict__ wq, const void* __restrict__ wo, void* __restrict__ out, char* ws) {
    extern __shared__ __attribute__((aligned(16))) char smem[];
    LAS char* L = (LAS char*)smem;
    const unsigned Lb = (unsigned)(size_t)L;
    const int tid = threadIdx.x, lane = tid & 63, w = rfl(tid >> 6);
    const int xcd = blockIdx.x & 7, idx = blockIdx.x >> 3;
    const int hl = idx & 3, b = idx >> 2, h = xcd * 4 + hl, bh = b * 32 + h;
    const int wgid = bh;
    STAMP_DECL
    STAMP(0);
    const __amdgpu_buffer_rsrc_t rws = mkrsrc(ws, WS_BYTES);
    const int cb0 = ((b >> 1) + hl) & 3, rotp = b % 3;
    u64 tag = 0;
    constexpr int npre = 0;
    const int pos = lane & 15, rq = lane >> 4;
    auto wq_off = [&](int n) -> int { return ((((n >> 2) + cb0) & 3) * 128 + ((n & 3) ^ (2 * (b & 1))) * 32) * 8192 + h * 256; };
    {
        unsigned long long pc; int len;
        asm volatile("s_getpc_b64 %0\n.Lfk_begin:\n\ts_mov_b32 %1, .Lfk_end-.Lfk_begin" : "=s"(pc), "=s"(len));
        __builtin_amdgcn_raw_ptr_buffer_load_lds(mkrsrc((const void*)pc, len), (LAS void*)(L + CTL + 256), 4, lane * 4, (idx * 5 + w) * 256, 0, 0);
    }
    {
        const bool isx = w < 3;
        const int sh = isx ? 10 : 13;
        const __amdgpu_buffer_rsrc_t r0 = mkrsrc(isx ? (const char*)x + (size_t)b * 98304 : (const char*)wq, isx ? 98304 : 512 * 8192);
        int vo[4];
#pragma unroll
        for (int k = 0; k < 4; ++k) vo[k] = ((4 * k + rq) << sh) + 16 * (pos ^ (rq << 2) ^ k) - 1024 * k;
        dma_chunk<0>(r0, L + (isx ? X_BASE + w * SLOT : P_BASE + (w - 3) * SLOT), vo, isx ? w * 32768 + cb0 * 256 : wq_off(w - 3), 16 << sh);
    }

    if (w >= 3) {
        int vo1k[4];
#pragma unroll
        for (int k = 0; k < 4; ++k) vo1k[k] = (4 * k + rq) * 1024 + 16 * (pos ^ (rq << 2) ^ k) - 1024 * k;
        const __amdgpu_buffer_rsrc_t rwo = mkrsrc(wo, 4096 * 1024);
        auto wo_off = [&](int ci) -> int {
            int head = hl;
            if (ci >= 4) { int ps = (ci >> 2) - 1 + rotp; ps = ps >= 3 ? ps - 3 : ps; head = (hl + 1 + ps) & 3; }
            return ((xcd * 4 + head) * 128 + (ci & 3) * 32) * 1024 + hl * 256;
        };
        __builtin_amdgcn_s_setprio(LD_PRIO);
        unsigned prog = 0;
        unsigned fulla = Lb + CTL + (w == 3 ? C_FULLK : C_FULLW);
        OPAQUE(fulla);
        auto gate = [&](unsigned need) {
            if (__builtin_expect(prog < need, 0)) {
                for (unsigned sp = 0; prog < need && sp < SPIN_CAP; ++sp) { if (sp) __builtin_amdgcn_s_sleep(LD_SLEEP); prog = lds_min3(Lb + CTL + C_G); }
            }
        };
        if (w == 3) {
            const __amdgpu_buffer_rsrc_t rx = mkrsrc((const char*)x + (size_t)b * 98304, 98304);
            const __amdgpu_buffer_rsrc_t rk = mkrsrc((const char*)K + (size_t)bh * 131072, 131072);
            const __amdgpu_buffer_rsrc_t rv = mkrsrc((const char*)V + (size_t)bh * 131072, 131072);
            int vo256[4];
#pragma unroll
            for (int k = 0; k < 4; ++k) vo256[k] = rq * 256 + 16 * (pos ^ (rq << 2) ^ k);
            const int trot = TROT;
            tag = __hip_atomic_load((GAS u64*)(ws + WS_EPOCH + b * 256), RLX_AGENT) + 1;
#pragma unroll
            for (int i = 3; i < 12; ++i) {
                const int cbi = i / 3, r = i % 3;
                if (i >= 3 + X_D) { VMCNT(8 * (X_D - 1)); lds_st(fulla, npre + i - X_D + 1); }
                if (i == 9) gate(1);
                dma_chunk<0>(rx, L + (cbi == 1 ? S_BASE + 2 * SLOT : (cbi == 2 ? S_BASE + 5 * SLOT : X_BASE)) + r * SLOT, vo1k, r * 32768 + ((cbi + cb0) & 3) * 256, 16384);
                if (i == 3 + X_D - 1) { VMCNT(8 * X_D); __builtin_amdgcn_s_barrier(); OPAQUE(tag); }
            }
            unsigned pubd = npre + 12 - X_D;
            prog = lds_min3(Lb + CTL + C_G);
            if (prog < KV_START) {
#pragma unroll
                for (int d = X_D - 1; d >= 0; --d) { VMCNT(8 * d); lds_st(fulla, npre + 12 - d); }
                pubd = npre + 12;
                gate(KV_START);
            }
            STAMP(1);
#pragma unroll 1
            for (int p = npre >> 1; p < 16; ++p) {
                const int c = 2 * p;
                LAS char* sl = L + ((c & 8) ? S_BASE : P_BASE) + (c & 7) * SLOT;
                VMCNT(8 * (KV_D - 1)); lds_st(fulla, max(pubd, (unsigned)(12 + c - KV_D + 1)));
                gate(c + 1);
                if (p == 0 || p == 13) dma_chunk<KV_AUX>(rk, sl, vo256, (((p ? 14 : 0) + trot) & 15) * 8192, 4096);
                else dma_chunk<KV_AUX>(rv, sl, vo256, (((p < 13 ? p - 1 : 2 * p - 16) + trot) & 15) * 8192, 4096);
                VMCNT(8 * (KV_D - 1)); lds_st(fulla, max(pubd, (unsigned)(12 + c + 1 - KV_D + 1)));
                gate(c + 2);
                if (p < 14) dma_chunk<KV_AUX>(rk, sl + SLOT, vo256, (((p < 13 ? p + 1 : 15) + trot) & 15) * 8192, 4096);
                else dma_chunk<KV_AUX>(rv, sl + SLOT, vo256, ((2 * p - 15 + trot) & 15) * 8192, 4096);
            }
            STAMP(2);
#pragma unroll
            for (int d = KV_D - 1; d >= 0; --d) { VMCNT(8 * d); lds_st(fulla, 12 + 32 - d); }
            STAMP(3);
            {
                int ps = (lane & 3) + rotp; ps = ps >= 3 ? ps - 3 : ps;
                const int pw = b * 32 + xcd * 4 + ((hl + 1 + ps) & 3);
                GAS u64* fp = (GAS u64*)(ws + WS_F1 + (pw * 3 + min((lane >> 2) & 3, 2)) * 128);
                unsigned m = 0;
                for (unsigned sp = 0; m != 0x777u; ++sp) {
                    const u64 f = __hip_atomic_load(fp, RLX_AGENT);
                    unsigned r = (unsigned)__builtin_amdgcn_ballot_w64(f == tag) & 0x777u;
                    if (sp >= SPIN_CAP) r = 0x777u;
                    if (r != m) { m = r; lds_st(Lb + CTL + C_RDY, m); }
                }
            }
            STAMP(4);
        } else {
            const __amdgpu_buffer_rsrc_t rwq = mkrsrc(wq, 512 * 8192);
            int vo8k[4];
#pragma unroll
            for (int k = 0; k < 4; ++k) vo8k[k] = (4 * k + rq) * 8192 + 16 * (pos ^ (rq << 2) ^ k) - 1024 * k;
#pragma unroll
            for (int n = 2; n < 16; ++n) {
                if (n >= 2 + W_D) { VMCNT(8 * (W_D - 1)); lds_st(fulla, n - W_D + 1); }
                if (n == 10) gate(XS1 + 2);
                if (n == 13) gate(XS2 + 2);
                dma_chunk<0>(rwq, L + (n & 8 ? S_BASE : P_BASE) + (n & 7) * SLOT, vo8k, wq_off(n), 131072);
                if (n == 3) { VMCNT(16); __builtin_amdgcn_s_barrier(); }
            }
            STAMP(1);
#pragma unroll
            for (int d = W_D - 1; d >= 0; --d) { VMCNT(8 * d); lds_st(fulla, 16 - d); }
            int vow[4];
#pragma unroll
            for (int k = 0; k < 4; ++k) vow[k] = (4 * k + rq) * 1024 + 16 * (pos ^ (rq << 1)) - 1024 * k;
#pragma unroll 1
            for (int i = 0; i < 16; ++i) {
                const int ci = i < 3 ? 8 + i : (i < 11 ? i - 3 : i);
                gate(i < 3 ? WO_START : 30 + i);
                dma_chunk<0>(rwo, L + ci * SLOT, vow, wo_off(ci), 16384);
                VMCNT(8 * (WO_D - 1)); if (i + 2 > WO_D) lds_st(fulla, 16 + i + 2 - WO_D);
            }
#pragma unroll
            for (int d = WO_D - 2; d >= 0; --d) { VMCNT(8 * d); lds_st(fulla, 32 - d); }
            STAMP(2);
            STAMP(3);
        }
    } else {
        const int t = w;
        if (tid < 64) lds_st(Lb + CTL + tid * 4, 0u);
        tag = __hip_atomic_load((GAS u64*)(ws + WS_EPOCH + b * 256), RLX_AGENT) + 1;
        const int r32 = lane & 31, hh = lane >> 5;
        volatile LAS unsigned* ctl = (volatile LAS unsigned*)(L + CTL);
        const int fr_ = ((r32 & 3) << 2) | ((r32 >> 2) & 3);
        unsigned rbx = Lb + t * SLOT + ((256 * r32 + 16 * (hh ^ fr_)) ^ (128 * (b & 1)));
        const int pr = (r32 & 19) | ((r32 & 4) << 1) | ((r32 & 8) >> 1);
        const int fp_ = ((pr & 3) << 2) | ((pr >> 2) & 3);
        unsigned rbk = Lb + P_BASE + 256 * pr + 16 * (hh ^ fp_);
        const int blk = (lane >> 4) & 1, q4 = (lane & 15) >> 2, p4 = lane & 3;
        unsigned tb0 = Lb + 256 * (8 * hh + q4) + 64 * q4 + 16 * ((2 * blk + (p4 & 1)) ^ (2 * hh)) + 8 * (p4 >> 1);
        unsigned availW = 2, availK = 0;
        auto waitW = [&](unsigned need) { if (__builtin_expect(availW < need, 0)) for (unsigned sp = 0; availW < need && sp < SPIN_CAP; ++sp) { if (sp) __builtin_amdgcn_s_sleep(CP_SLEEP); availW = rfl(ctl[C_FULLW / 4]); } CBAR(); };
        auto waitKs = [&](unsigned need) { if (__builtin_expect(availK < need, 0)) for (unsigned sp = 0; availK < need && sp < SPIN_CAP; ++sp) { if (sp) __builtin_amdgcn_s_sleep(CP_SLEEP); availK = rfl(ctl[C_FULLK / 4]); } CBAR(); };
        auto waitK = [&](unsigned n) { waitKs(12 + n); };
        f32x16 acc[4];
#pragma unroll
        for (int d = 0; d < 4; ++d) acc[d] = f32x16{};
        {
            v4u fa[2][4], fb[2][4], xf[32];
            const unsigned tbw0 = tb0 + P_BASE, tbw1 = tb0 + S_BASE;
            asm volatile("s_waitcnt vmcnt(0) lgkmcnt(0)" ::: "memory");
            __builtin_amdgcn_s_barrier();
            CBAR();
            STAMP(14);
            xf[0] = row_rd(rbx + X_BASE); xf[1] = row_rd((rbx + X_BASE) ^ 32u);
            tr_chunk(tbw0, 0, fa);
#pragma unroll
            for (int ks = 2; ks < 8; ++ks) xf[ks] = row_rd(((rbx + X_BASE) ^ (unsigned)(32 * ks)));
            ctl[C_G / 4 + t] = 1;
#pragma unroll
            for (int n = 0; n < 16; ++n) {
                const int xq = n == XS1 ? 1 : (n == XS2 ? 2 : (n == XS3 ? 3 : 0));
                if (n & 1) waitW(n + 3 < 16 ? n + 3 : 16);
                if (xq) waitKs(npre + 3 * xq + t + 1);
                const int offn = ((n + 1) & 7) * SLOT;
                const unsigned tbw = n + 1 < 8 ? tbw0 : tbw1;
                const unsigned xn = rbx + (xq == 1 ? S_BASE + 2 * SLOT : (xq == 2 ? S_BASE + 5 * SLOT : X_BASE));
                v2u fw;
                SB();
#pragma unroll
                for (int g = 0; g < 8; ++g) {
                    if (n & 1) { acc[g >> 1] = mfma32(fb[g & 1][g >> 1], xf[2 * n + (g & 1)], acc[g >> 1]); if (n < 15) tr_frag_g(tbw, offn, g, fa); }
                    else { acc[g >> 1] = mfma32(fa[g & 1][g >> 1], xf[2 * n + (g & 1)], acc[g >> 1]); tr_frag_g(tbw, offn, g, fb); }
                    if (xq) xf[8 * xq + g] = row_rd(xn ^ (unsigned)(32 * g));
                    if (g == 3 && !(n & 1)) fw = *(volatile LAS v2u*)(L + CTL);
                    if (g == 0 && !(n & 1) && n > 0) ctl[C_G / 4 + t] = n + 1;
                    if (g == 7 && n == 14) ctl[C_G / 4 + t] = 16;
                    if (g == 7 && !(n & 1)) { availK = rfl(fw[0]); availW = max(2u, rfl(fw[1])); }
                    SB();
                }
            }
        }
        asm volatile("" :: "v"(tag));
        STAMP(1);
        constexpr float CS = 0.08838834764831845f * 1.4426950408889634f;
        constexpr float THR = 8.0f;
        v4u qf[8];
#pragma unroll
        for (int d = 0; d < 4; ++d) { acc[d] *= CS; qf[2 * d] = pack8(acc[d], 0); qf[2 * d + 1] = pack8(acc[d], 1); }
#pragma unroll
        for (int d = 0; d < 4; ++d) acc[d] = f32x16{};
        float lsum = 0.f;
        f32x16 sA, sB, negm; v4u kf[8], vf[2][4];
        unsigned tbk = tb0 + P_BASE; OPAQUE(tbk);
        unsigned ctla = Lb + CTL + C_FULLK, pka = Lb + CTL + C_G + 4 * t, relv = 18;
        OPAQUE(ctla); OPAQUE(pka); OPAQUE(relv);
        auto decide = [&](float tm, f32x16& nxt) {
            if (__builtin_expect(__builtin_amdgcn_ballot_w64(tm > THR) != 0ull, 0)) {
                const float dl = fmaxf(swapmax(tm), 0.f), f = __builtin_amdgcn_exp2f(-dl);
                lsum *= f;
#pragma unroll
                for (int d = 0; d < 4; ++d) acc[d] *= f;
#pragma unroll
                for (int i = 0; i < 16; ++i) { nxt[i] -= dl; negm[i] -= dl; }
            }
        };
        auto block = [&](f32x16& cur, f32x16& nxt, int vslot, auto KR) -> float {
            v4u p[2];
            float m = 0.f, m1 = 0.f, s0 = 0.f, s1 = 0.f; unsigned fk = 0;
            SB();
#pragma unroll
            for (int g = 0; g < 8; ++g) {
                nxt = mfma32(kf[g], qf[g], g == 0 ? negm : nxt);
                tr_frag_g(tbk, vslot * SLOT, g, vf);
                const int e0 = (3 * g + 1) / 2, e1 = (3 * (g + 1) + 1) / 2;
#pragma unroll
                for (int e = e0; e < e1; ++e) cur[e] = __builtin_amdgcn_exp2f(cur[e]);
                if (g >= 1) {
                    const int pp0 = ((3 * (g - 1) + 1) / 2) / 2, pp1 = e0 / 2;
#pragma unroll
                    for (int q = pp0; q < pp1; ++q) p[q >> 2][q & 3] = cvtpk(cur[2 * q], cur[2 * q + 1]);
                }
                SB();
            }
#pragma unroll
            for (int g = 0; g < 8; ++g) {
                acc[g & 3] = mfma32(vf[g >> 2][g & 3], p[g >> 2], acc[g & 3]);
                if (KR && g < 4) { kf[2 * g] = row_rd((rbk ^ (unsigned)(64 * g)) + (vslot + 1) * SLOT); kf[2 * g + 1] = row_rd((rbk ^ (unsigned)(64 * g + 32)) + (vslot + 1) * SLOT); }
                if (g == 0) { cur[12] = __builtin_amdgcn_exp2f(cur[12]); cur[13] = __builtin_amdgcn_exp2f(cur[13]); p[1][1] = cvtpk(cur[10], cur[11]); }
                if (g == 1) { cur[14] = __builtin_amdgcn_exp2f(cur[14]); cur[15] = __builtin_amdgcn_exp2f(cur[15]); p[1][2] = cvtpk(cur[12], cur[13]); }
                if (g == 2) { p[1][3] = cvtpk(cur[14], cur[15]); m = max3f(nxt[0], nxt[1], nxt[2]); m1 = max3f(nxt[3], nxt[4], nxt[5]); }
                if (g == 3) { m = max3f(m, nxt[6], nxt[7]); m1 = max3f(m1, nxt[8], nxt[9]); }
                if (g == 4) { m = max3f(m, nxt[10], nxt[11]); m1 = max3f(m1, nxt[12], nxt[13]); relv += KR ? 2 : 1; *(volatile LAS unsigned*)(size_t)pka = relv; fk = *(volatile LAS unsigned*)(size_t)ctla; }
                if (g == 5) { m = max3f(m, nxt[14], nxt[15]); m = max3f(m, m1, m1); }
                if (g >= 4) {
#pragma unroll
                    for (int e = 4 * (g - 4); e < 4 * (g - 4) + 4; e += 2) { s0 = fadd_s(s0, cur[e]); s1 = fadd_s(s1, cur[e + 1]); }
                }
                SB();
            }
            lsum += s0 + s1;
            OPAQUE(lsum);
            availK = rfl(fk);
            return m;
        };
        waitK(1);
#pragma unroll
        for (int ks = 0; ks < 8; ++ks) kf[ks] = row_rd(rbk ^ (unsigned)(32 * ks));
#pragma unroll
        for (int ks = 0; ks < 8; ++ks) sA = mfma32(kf[ks], qf[ks], ks == 0 ? f32x16{} : sA);
        waitK(2);
#pragma unroll
        for (int ks = 0; ks < 8; ++ks) kf[ks] = row_rd((rbk ^ (unsigned)(32 * ks)) + SLOT);
        *(volatile LAS unsigned*)(size_t)pka = relv;
        {
            float m = fmaxf(sA[0], sA[1]);
#pragma unroll
            for (int i = 2; i < 16; i += 2) m = fmaxf(fmaxf(m, sA[i]), sA[i + 1]);
            m = swapmax(m);
#pragma unroll
            for (int i = 0; i < 16; ++i) { sA[i] -= m; negm[i] = -m; }
            OPAQUE(negm);
        }
        STAMP(2);
        {
            constexpr std::integral_constant<bool, true> T{};
            constexpr std::integral_constant<bool, false> F{};
#pragma unroll 1
            for (int j = 0; j < 12; j += 4) {
                waitK(2 * j + 4); decide(block(sA, sB, 2, T), sB);
                waitK(2 * j + 6); decide(block(sB, sA, 4, T), sA);
                waitK(2 * j + 8); decide(block(sA, sB, 6, T), sB);
                tbk ^= S_BASE; rbk ^= S_BASE;
                waitK(2 * j + 10); decide(block(sB, sA, 0, T), sA);
            }
        }
        v4u pe[4][2], cw;
        {
            auto estep = [&](int e, f32x16& cur, f32x16& nxt) {
                float s0 = 0.f, s1 = 0.f;
                SB();
#pragma unroll
                for (int g = 0; g < 8; ++g) {
                    if (e < 3) nxt = mfma32(kf[g], qf[g], g == 0 ? negm : nxt);
                    if (e < 2) kf[g] = row_rd((rbk ^ (unsigned)(32 * g)) + (2 + e) * SLOT);
                    cur[2 * g] = __builtin_amdgcn_exp2f(cur[2 * g]); cur[2 * g + 1] = __builtin_amdgcn_exp2f(cur[2 * g + 1]);
                    if (e == 3 && g == 2) cw = *(volatile LAS v4u*)(size_t)ctla;
                    if (g > 0) { pe[e][(g - 1) >> 2][(g - 1) & 3] = cvtpk(cur[2 * g - 2], cur[2 * g - 1]); s0 += cur[2 * g - 2]; s1 += cur[2 * g - 1]; }
                    SB();
                }
                if (e < 2) { relv += 1; *(volatile LAS unsigned*)(size_t)pka = relv; }
                pe[e][1][3] = cvtpk(cur[14], cur[15]);
                lsum += (s0 + cur[14]) + (s1 + cur[15]);
                if (e < 3) {
                    float m = fmaxf(nxt[0], nxt[1]);
#pragma unroll
                    for (int i = 2; i < 16; i += 2) m = fmaxf(fmaxf(m, nxt[i]), nxt[i + 1]);
                    if (__builtin_expect(__builtin_amdgcn_ballot_w64(m > THR) != 0ull, 0)) {
                        const float dl = fmaxf(swapmax(m), 0.f), f = __builtin_amdgcn_exp2f(-dl);
                        lsum *= f;
#pragma unroll
                        for (int d = 0; d < 4; ++d) acc[d] *= f;
#pragma unroll
                        for (int i = 0; i < 16; ++i) { nxt[i] -= dl; negm[i] -= dl; }
#pragma unroll
                        for (int q = 0; q <= e; ++q)
#pragma unroll
                            for (int i = 0; i < 8; ++i) { const unsigned u = pe[q][i >> 2][i & 3]; pe[q][i >> 2][i & 3] = cvtpk(__uint_as_float(u << 16) * f, __uint_as_float(u & 0xffff0000u) * f); }
                    }
                }
            };
            waitK(27); estep(0, sA, sB);
            waitK(28); estep(1, sB, sA);
            estep(2, sA, sB);
            estep(3, sB, sA);
            STAMP(3);
        }
        const int l16 = lane * 16;
        v4u pf[3][8];
        unsigned pend = 7, nas = 0, grp[3] = {0, 0, 0};
        auto rdy = [&]() -> unsigned { const unsigned r = (rfl(ctl[C_RDY / 4]) >> (4 * t)) & pend; CBAR(); return r; };
        auto take = [&](int k, unsigned r) {
            const unsigned g = __builtin_ctz(r);
            pend &= ~(1u << g); grp[k] = g; nas = k + 1;
            int ps = g + rotp; ps = ps >= 3 ? ps - 3 : ps;
            const int so = (int)WS_O + (b * 32 + xcd * 4 + ((hl + 1 + ps) & 3)) * 24576 + t * 8192;
#pragma unroll
            for (int i = 0; i < 8; ++i) pf[k][i] = __builtin_amdgcn_raw_buffer_load_b128(rws, l16 + (i & 3) * 1024, so + (i >> 2) * 4096, 16);
        };
        auto must = [&](int k) { unsigned r = rdy(); for (unsigned sp = 0; r == 0u; ++sp) { r = rdy(); if (sp > 4 * SPIN_CAP) r = pend; } take(k, r); };
        availK = rfl(cw[0]); availW = rfl(cw[1]);
        float inv = __builtin_amdgcn_rcpf(swapsum(lsum));
        OPAQUE(inv);
        unsigned tbo = Lb + 256 * (8 * hh + q4) + 16 * ((8 * (p4 & 1) + blk) ^ (q4 << 1)) + 8 * (p4 >> 1);
        OPAQUE(tbo);
        v4u fa[2][4], fb[2][4];
        waitW(20);
        waitK(29);
        tr_chunk(tbk, 4 * SLOT, fa);
#pragma unroll
        for (int e = 0; e < 4; ++e) {
            if (e < 3) waitK(30 + e);
            unsigned fk = 0;
            SB();
#pragma unroll
            for (int g = 0; g < 8; ++g) {
                if (e & 1) { acc[g >> 1] = mfma32(fb[g & 1][g >> 1], pe[e][g & 1], acc[g >> 1]); if (e < 3) tr_frag_g(tbk, (5 + e) * SLOT, g, fa); else tro_frag_g(tbo, 0, g, fa); }
                else { acc[g >> 1] = mfma32(fa[g & 1][g >> 1], pe[e][g & 1], acc[g >> 1]); tr_frag_g(tbk, (5 + e) * SLOT, g, fb); }
                if (g == 0 && e == 0) { relv += 1; *(volatile LAS unsigned*)(size_t)pka = relv; }
                if (g == 4 && e < 2) fk = *(volatile LAS unsigned*)(size_t)ctla;
                if (g == 2 && e == 3) cw = *(volatile LAS v4u*)(size_t)ctla;
                if (g == 7 && e < 2) availK = rfl(fk);
                SB();
            }
        }
        STAMP(4);
        availW = rfl(cw[1]);
        const unsigned r0 = (rfl(cw[2]) >> (4 * t)) & 7u;
        CBAR();
        v4u cur[8];
        {
            const __amdgpu_buffer_rsrc_t ro = mkrsrc(ws + WS_O + wgid * 24576 + t * 8192, 8192);
#pragma unroll
            for (int d = 0; d < 4; ++d) {
                acc[d] *= inv; cur[2 * d] = pack8(acc[d], 0); cur[2 * d + 1] = pack8(acc[d], 1);
                __builtin_amdgcn_raw_buffer_store_b128(cur[2 * d], ro, l16 + (2 * d) * 1024, 0, 16);
                __builtin_amdgcn_raw_buffer_store_b128(cur[2 * d + 1], ro, l16 + (2 * d + 1) * 1024, 0, 16);
            }
        }
#pragma unroll
        for (int d = 0; d < 4; ++d) acc[d] = f32x16{};
        STAMP(5);
        { unsigned r = r0; if (r) { take(0, r); r &= pend; if (r) take(1, r); } }
        auto gemm4 = [&](unsigned tb, const v4u* cur, unsigned tbn, auto FIRST, auto LAST) {
            auto flag = [&]() {
                VMCNT(0);
                if (lane == 0) __hip_atomic_store((GAS u64*)(ws + WS_F1 + (wgid * 3 + t) * 128), tag, RLX_AGENT);
                SB();
            };
            OPAQUE(tb);
            SB();
            gemm_step<true>(acc, fa, cur[0], cur[1], tb, SLOT, fb);
            gemm_step<true>(acc, fb, cur[2], cur[3], tb, 2 * SLOT, fa);
            if (FIRST && F1_POS == 2) flag();
            gemm_step<true>(acc, fa, cur[4], cur[5], tb, 3 * SLOT, fb);
            if (FIRST && F1_POS == 3) flag();
            if (!LAST) gemm_step<true>(acc, fb, cur[6], cur[7], tbn, 0, fa);
            else gemm_step<false>(acc, fb, cur[6], cur[7], 0, 0, fa);
            if (FIRST && F1_POS == 4) flag();
        };
        auto gbase = [&](int k) -> unsigned { return tbo + (grp[k] + 1) * (4 * SLOT); };
        auto wneed = [&](int k) -> unsigned { return grp[k] == 0 ? 27u : (grp[k] == 1 ? 28u : 32u); };
        auto refill = [&](int k) {
            waitW(wneed(k));
            const unsigned tb = gbase(k);
#pragma unroll
            for (int g = 0; g < 8; ++g) tro_frag_g(tb, 0, g, fa);
        };
        {
            constexpr std::integral_constant<bool, true> T{};
            constexpr std::integral_constant<bool, false> F{};
            waitW(23);
            bool pre = nas >= 1 && availW >= wneed(0);
            gemm4(tbo, cur, gbase(0), T, F);
            STAMP(6);
            if (nas < 1) must(0);
            if (!pre) refill(0);
            { unsigned r = rdy(); if (nas == 1 && r) { take(1, r); r &= pend; } if (nas == 2 && r) take(2, r); }
            STAMP(11);
            if (availW < 32u) availW = rfl(ctl[C_FULLW / 4]);
            pre = nas >= 2 && availW >= wneed(1);
            gemm4(gbase(0), pf[0], gbase(1), F, F);
            if (nas < 2) must(1);
            if (!pre) refill(1);
            if (nas == 2) { unsigned r = rdy(); if (r) take(2, r); }
            STAMP(12);
            if (availW < 32u) availW = rfl(ctl[C_FULLW / 4]);
            pre = nas >= 3 && availW >= wneed(2);
            gemm4(gbase(1), pf[1], gbase(2), F, F);
            if (nas < 3) must(2);
            if (!pre) refill(2);
            STAMP(13);
            gemm4(gbase(2), pf[2], 0, F, T);
        }
        STAMP(7);
        {
            const __amdgpu_buffer_rsrc_t rp = mkrsrc(ws + WS_P + wgid * 24576 + t * 8192, 8192);
#pragma unroll
            for (int d = 0; d < 4; ++d) {
                __builtin_amdgcn_raw_buffer_store_b128(pack8(acc[d], 0), rp, l16 + (2 * d) * 1024, 0, 16);
                __builtin_amdgcn_raw_buffer_store_b128(pack8(acc[d], 1), rp, l16 + (2 * d + 1) * 1024, 0, 16);
            }
        }
        ctl[C_DONE / 4] = 1;
        VMCNT(0);
        if (lane < 2) __hip_atomic_store((GAS u64*)(ws + WS_F2 + ((wgid * 3 + t) * 2 + lane) * 128), tag, RLX_AGENT);
        STAMP(8);
    }
    if (xcd < 6 && w < 4) {
        const int t = xcd >> 1, f = 4 * (xcd & 1) + w;
        if (w == 3) {
            for (unsigned sp = 0; rfl(lds_ld(Lb + CTL + C_DONE)) == 0u && sp < SPIN_CAP; ++sp) __builtin_amdgcn_s_sleep(2);
            GAS u64* fp = (GAS u64*)(ws + WS_F2 + (((b * 32 + (lane & 7) * 4 + hl) * 3 + t) * 2 + (xcd & 1)) * 128);
            for (unsigned sp = 0; sp < SPIN_CAP; ++sp) {
                const u64 fl = __hip_atomic_load(fp, RLX_AGENT);
                if (__builtin_amdgcn_ballot_w64(fl != tag) == 0ull) break;
            }
            lds_st(Lb + CTL + C_READY, 1);
            if (xcd == 0 && hl == 0 && lane == 0) __hip_atomic_store((GAS u64*)(ws + WS_EPOCH + b * 256), tag, RLX_AGENT);
        } else {
            for (unsigned sp = 0; rfl(lds_ld(Lb + CTL + C_READY)) == 0u && sp < SPIN_CAP; ++sp) {}
        }
        CBAR();
        STAMP(9);
        v4u pp[8];
#pragma unroll
        for (int xx = 0; xx < 8; ++xx) pp[xx] = __builtin_amdgcn_raw_buffer_load_b128(rws, lane * 16, (int)WS_P + (b * 32 + xx * 4 + hl) * 24576 + t * 8192 + f * 1024, 16);
        float s[8];
#pragma unroll
        for (int k = 0; k < 8; ++k) s[k] = 0.f;
#pragma unroll
        for (int xx = 0; xx < 8; ++xx)
#pragma unroll
            for (int k = 0; k < 4; ++k) { s[2 * k] += __uint_as_float(pp[xx][k] << 16); s[2 * k + 1] += __uint_as_float(pp[xx][k] & 0xffff0000u); }
        v4u o = {cvtpk(s[0], s[1]), cvtpk(s[2], s[3]), cvtpk(s[4], s[5]), cvtpk(s[6], s[7])};
        *(v4u*)((char*)out + ((size_t)(b * 96 + 32 * t + (lane & 31)) * 512 + 128 * hl + 64 * (lane >> 5) + 8 * f) * 2) = o;
        STAMP(10);
    }
    STAMP_FLUSH(ws);
    asm volatile(".Lfk_end:");
}

extern "C" void kernel_launch(void* const* d_in, const int* in_sizes, int n_in, void* d_out, int out_size, void* d_ws, size_t ws_size, hipStream_t stream) {
    (void)hipFuncSetAttribute((const void*)fused_kernel, hipFuncAttributeMaxDynamicSharedMemorySize, LDS_BYTES);
    fused_kernel<<<256, 320, LDS_BYTES, stream>>>(d_in[0], d_in[1], d_in[2], d_in[3], d_in[4], d_out, (char*)d_ws);
}
```

```cpp
#define TLB2 1
#include <hip/hip_runtime.h>
#include <hip/hip_bf16.h>
#include <type_traits>

typedef unsigned v4u __attribute__((ext_vector_type(4)));
typedef unsigned v2u __attribute__((ext_vector_type(2)));
typedef float f32x16 __attribute__((ext_vector_type(16)));
typedef float f32x2 __attribute__((ext_vector_type(2)));
typedef __bf16 bf16x8 __attribute__((ext_vector_type(8)));
typedef __bf16 bf16x2 __attribute__((ext_vector_type(2)));
typedef short s16x4 __attribute__((ext_vector_type(4)));
typedef unsigned long long u64;
#define LAS __attribute__((address_space(3)))
#define GAS __attribute__((address_space(1)))
#define DI __device__ __forceinline__
#define RLX_AGENT __ATOMIC_RELAXED, __HIP_MEMORY_SCOPE_AGENT

#define KV_AUX 2
#define KV_D 5
#define W_D 3
#define CLR_SLEEP 4
#define LD_SLEEP 1
#define CP_SLEEP 1
#define KV_PRE 0
#define X_D 3
#define LD_PRIO 3
#define TROT idx
#define F1_EARLY 1
#define P_SLEEP 0
#define CODE_PF 1
#define FK_G 10
#define POLL_AT 2
#define RDY_FRESH 1
#define TAKE3 1
#define XS1 1
#define XS2 3
#define XS3 9
#define TYPEB 0
#define WO_START 24
#define WO_D 5
#define KV_START 1

#define STAMP(i)
#define STAMP_DECL
#define STAMP_FLUSH(ws)

constexpr int SLOT = 8192;
constexpr int P_BASE = 0, X_BASE = 8 * SLOT, S_BASE = 11 * SLOT, CTL = 19 * SLOT;
constexpr int LDS_BYTES = CTL + 512;
constexpr int C_FULLK = 0, C_FULLW = 4, C_G = 16  , C_READY = 64, C_DONE = 68, C_RDY = 8;
constexpr size_t WS_EPOCH = 0;
constexpr size_t WS_F1 = 4096;
constexpr size_t WS_F2 = WS_F1 + 768 * 128;
constexpr size_t WS_O = WS_F2 + 1536 * 128;
constexpr size_t WS_P = WS_O + 256 * 24576;
constexpr int WS_BYTES = (int)(WS_P + 256 * 24576);
constexpr unsigned SPIN_CAP = 1u << 22;

DI f32x16 mfma32(v4u a, v4u b, f32x16 c) {
    return __builtin_amdgcn_mfma_f32_32x32x16_bf16(__builtin_bit_cast(bf16x8, a), __builtin_bit_cast(bf16x8, b), c, 0, 0, 0);
}
DI unsigned cvtpk(float lo, float hi) { f32x2 v = {lo, hi}; bf16x2 b = __builtin_convertvector(v, bf16x2); return __builtin_bit_cast(unsigned, b); }
DI __amdgpu_buffer_rsrc_t mkrsrc(const void* p, int bytes) { return __builtin_amdgcn_make_buffer_rsrc((void*)p, 0, bytes, 0x00020000); }
DI unsigned rfl(unsigned x) { return __builtin_amdgcn_readfirstlane(x); }
DI v4u pack8(const f32x16& a, int s) {
    return v4u{cvtpk(a[8 * s], a[8 * s + 1]), cvtpk(a[8 * s + 2], a[8 * s + 3]), cvtpk(a[8 * s + 4], a[8 * s + 5]), cvtpk(a[8 * s + 6], a[8 * s + 7])};
}
DI float swapmax(float x) { auto r = __builtin_amdgcn_permlane32_swap(__float_as_uint(x), __float_as_uint(x), false, false); return fmaxf(__uint_as_float(r[0]), __uint_as_float(r[1])); }
DI float swapsum(float x) { auto r = __builtin_amdgcn_permlane32_swap(__float_as_uint(x), __float_as_uint(x), false, false); return __uint_as_float(r[0]) + __uint_as_float(r[1]); }
DI float max3f(float a, float b, float c) { float r; asm("v_max3_f32 %0, %1, %2, %3" : "=v"(r) : "v"(a), "v"(b), "v"(c)); return r; }
DI float fadd_s(float a, float b) { float r; asm("v_add_f32_e32 %0, %1, %2" : "=v"(r) : "v"(a), "v"(b)); return r; }
#define SB() __builtin_amdgcn_sched_barrier(0)
#define OPAQUE(x) asm volatile("" : "+v"(x))
#define CBAR() asm volatile("" ::: "memory")
#define VMCNT(n) asm volatile("s_waitcnt vmcnt(%0)" :: "i"(n) : "memory")
#define LGKM0() asm volatile("s_waitcnt lgkmcnt(0)" ::: "memory")

DI unsigned lds_ld(unsigned addr) { unsigned r; asm volatile("ds_read_b32 %0, %1\n\ts_waitcnt lgkmcnt(0)" : "=v"(r) : "v"(addr) : "memory"); return r; }
DI void lds_st(unsigned addr, unsigned v) { asm volatile("ds_write_b32 %0, %1" :: "v"(addr), "v"(v) : "memory"); }
DI unsigned lds_min3(unsigned addr) {
    unsigned a, b, c;
    asm volatile("ds_read_b32 %0, %3\n\tds_read_b32 %1, %3 offset:4\n\tds_read_b32 %2, %3 offset:8\n\ts_waitcnt lgkmcnt(0)" : "=&v"(a), "=&v"(b), "=&v"(c) : "v"(addr) : "memory");
    return rfl(min(a, min(b, c)));
}

template <int AUX> DI void dma_chunk(__amdgpu_buffer_rsrc_t rs, LAS char* slot, const int (&voff)[4], int soff, int half) {
    __builtin_amdgcn_raw_ptr_buffer_load_lds(rs, (LAS void*)slot, 16, voff[0], soff, 0, AUX);
    __builtin_amdgcn_raw_ptr_buffer_load_lds(rs, (LAS void*)slot, 16, voff[1], soff, 1024, AUX);
    __builtin_amdgcn_raw_ptr_buffer_load_lds(rs, (LAS void*)slot, 16, voff[2], soff, 2048, AUX);
    __builtin_amdgcn_raw_ptr_buffer_load_lds(rs, (LAS void*)slot, 16, voff[3], soff, 3072, AUX);
    __builtin_amdgcn_raw_ptr_buffer_load_lds(rs, (LAS void*)(slot + 4096), 16, voff[0], soff + half, 0, AUX);
    __builtin_amdgcn_raw_ptr_buffer_load_lds(rs, (LAS void*)(slot + 4096), 16, voff[1], soff + half, 1024, AUX);
    __builtin_amdgcn_raw_ptr_buffer_load_lds(rs, (LAS void*)(slot + 4096), 16, voff[2], soff + half, 2048, AUX);
    __builtin_amdgcn_raw_ptr_buffer_load_lds(rs, (LAS void*)(slot + 4096), 16, voff[3], soff + half, 3072, AUX);
}
DI v2u tr_rd(unsigned addr, int off) { return __builtin_bit_cast(v2u, __builtin_amdgcn_ds_read_tr16_b64_v4i16((LAS s16x4*)(size_t)(addr + off))); }
DI v4u row_rd(unsigned addr) { return *(LAS v4u*)(size_t)addr; }
DI v4u tr_frag(unsigned tb, int off, int s, int ct) {
    v2u l2 = tr_rd(tb ^ (unsigned)(64 * ct), off + s * 4096), h2 = tr_rd(tb ^ (unsigned)(64 * ct + 16), off + s * 4096 + 1024);
    return v4u{l2[0], l2[1], h2[0], h2[1]};
}
DI void tr_frag_g(unsigned tb, int off, int g, v4u (&fr)[2][4]) { fr[g & 1][g >> 1] = tr_frag(tb, off, g & 1, g >> 1); }
DI void tr_chunk(unsigned tb, int off, v4u (&fr)[2][4]) {
#pragma unroll
    for (int g = 0; g < 8; ++g) tr_frag_g(tb, off, g, fr);
}
DI v4u tr2(unsigned a0, unsigned a1, int off) { v2u l2 = tr_rd(a0, off), h2 = tr_rd(a1, off + 1024); return v4u{l2[0], l2[1], h2[0], h2[1]}; }
DI void tro_frag_g(unsigned tb, int off, int g, v4u (&fr)[2][4]) {
    const unsigned a = tb ^ (unsigned)(32 * (g >> 1));
    v2u l2 = tr_rd(a, off + (g & 1) * 4096), h2 = tr_rd(a, off + (g & 1) * 4096 + 1024);
    fr[g & 1][g >> 1] = v4u{l2[0], l2[1], h2[0], h2[1]};
}
template <bool NEXT> DI void gemm_step(f32x16 (&acc)[4], const v4u (&cur)[2][4], v4u b0, v4u b1, unsigned tb, int off, v4u (&nxt)[2][4]) {
#pragma unroll
    for (int g = 0; g < 8; ++g) {
        acc[g >> 1] = mfma32(cur[g & 1][g >> 1], (g & 1) ? b1 : b0, acc[g >> 1]);
        if (NEXT) tro_frag_g(tb, off, g, nxt);
        SB();
    }
}

__global__ __launch_bounds__(384, 2) void fused_kernel(const void* __restrict__ x, const void* __restrict__ K, const void* __restrict__ V,
                                                      const void* __restrict__ wq, const void* __restrict__ wo, void* __restrict__ out, char* ws) {
    extern __shared__ __attribute__((aligned(16))) char smem[];
    LAS char* L = (LAS char*)smem;
    const unsigned Lb = (unsigned)(size_t)L;
    const int tid = threadIdx.x, lane = tid & 63, w = rfl(tid >> 6);
    const int xcd = blockIdx.x & 7, idx = blockIdx.x >> 3;
    const int hl = idx & 3, b = idx >> 2, h = xcd * 4 + hl, bh = b * 32 + h;
    const int wgid = bh;
    STAMP_DECL
    STAMP(0);
    const __amdgpu_buffer_rsrc_t rws = mkrsrc(ws, WS_BYTES);
    const int cb0 = ((b >> 1) + hl) & 3, rotp = b % 3;
    u64 tag = 0;
    constexpr int npre = 0;
    const int pos = lane & 15, rq = lane >> 4;
    auto wq_off = [&](int n) -> int { return ((((n >> 2) + cb0) & 3) * 128 + ((n & 3) ^ (2 * (b & 1))) * 32) * 8192 + h * 256; };
    if (w == 5) {
        const char* p0 = (const char*)K + (size_t)bh * 131072; const char* p1 = (const char*)V + (size_t)bh * 131072;
        const char* p2 = (const char*)wo + (size_t)xcd * 524288; const char* p3 = ws + WS_O + (size_t)wgid * 24576;
        const char* p4 = ws + WS_P + (size_t)wgid * 24576; const char* p5 = (const char*)out + (size_t)b * 98304;
        unsigned j;
        asm volatile("s_load_dword %0, %1, 0\n\ts_load_dword %0, %2, 0\n\ts_load_dword %0, %3, 0\n\ts_load_dword %0, %4, 0\n\ts_load_dword %0, %5, 0\n\ts_load_dword %0, %6, 0\n\ts_waitcnt lgkmcnt(0)"
                     : "=&s"(j) : "s"(p0), "s"(p1), "s"(p2), "s"(p3), "s"(p4), "s"(p5) : "memory");
        return;
    }
    {
        unsigned long long pc; int len;
        asm volatile("s_getpc_b64 %0\n.Lfk_begin:\n\ts_mov_b32 %1, .Lfk_end-.Lfk_begin" : "=s"(pc), "=s"(len));
        __builtin_amdgcn_raw_ptr_buffer_load_lds(mkrsrc((const void*)pc, len), (LAS void*)(L + CTL + 256), 4, lane * 4, (idx * 5 + w) * 256, 0, 0);
    }
    {
        const bool isx = w < 3;
        const int sh = isx ? 10 : 13;
        const __amdgpu_buffer_rsrc_t r0 = mkrsrc(isx ? (const char*)x + (size_t)b * 98304 : (const char*)wq, isx ? 98304 : 512 * 8192);
        int vo[4];
#pragma unroll
        for (int k = 0; k < 4; ++k) vo[k] = ((4 * k + rq) << sh) + 16 * (pos ^ (rq << 2) ^ k) - 1024 * k;
        dma_chunk<0>(r0, L + (isx ? X_BASE + w * SLOT : P_BASE + (w - 3) * SLOT), vo, isx ? w * 32768 + cb0 * 256 : wq_off(w - 3), 16 << sh);
    }

    if (w >= 3) {
        int vo1k[4];
#pragma unroll
        for (int k = 0; k < 4; ++k) vo1k[k] = (4 * k + rq) * 1024 + 16 * (pos ^ (rq << 2) ^ k) - 1024 * k;
        const __amdgpu_buffer_rsrc_t rwo = mkrsrc(wo, 4096 * 1024);
        auto wo_off = [&](int ci) -> int {
            int head = hl;
            if (ci >= 4) { int ps = (ci >> 2) - 1 + rotp; ps = ps >= 3 ? ps - 3 : ps; head = (hl + 1 + ps) & 3; }
            return ((xcd * 4 + head) * 128 + (ci & 3) * 32) * 1024 + hl * 128;
        };
        __builtin_amdgcn_s_setprio(LD_PRIO);
        unsigned prog = 0;
        unsigned fulla = Lb + CTL + (w == 3 ? C_FULLK : C_FULLW);
        OPAQUE(fulla);
        auto gate = [&](unsigned need) {
            if (__builtin_expect(prog < need, 0)) {
                for (unsigned sp = 0; prog < need && sp < SPIN_CAP; ++sp) { if (sp) __builtin_amdgcn_s_sleep(LD_SLEEP); prog = lds_min3(Lb + CTL + C_G); }
            }
        };
        if (w == 3) {
            const __amdgpu_buffer_rsrc_t rx = mkrsrc((const char*)x + (size_t)b * 98304, 98304);
            const __amdgpu_buffer_rsrc_t rk = mkrsrc((const char*)K + (size_t)bh * 131072, 131072);
            const __amdgpu_buffer_rsrc_t rv = mkrsrc((const char*)V + (size_t)bh * 131072, 131072);
            int vo256[4];
#pragma unroll
            for (int k = 0; k < 4; ++k) vo256[k] = rq * 256 + 16 * (pos ^ (rq << 2) ^ k);
            const int trot = TROT;
            tag = __hip_atomic_load((GAS u64*)(ws + WS_EPOCH + b * 256), RLX_AGENT) + 1;
#pragma unroll
            for (int i = 3; i < 12; ++i) {
                const int cbi = i / 3, r = i % 3;
                if (i >= 3 + X_D) { VMCNT(8 * (X_D - 1)); lds_st(fulla, npre + i - X_D + 1); }
                if (i == 9) gate(1);
                dma_chunk<0>(rx, L + (cbi == 1 ? S_BASE + 2 * SLOT : (cbi == 2 ? S_BASE + 5 * SLOT : X_BASE)) + r * SLOT, vo1k, r * 32768 + ((cbi + cb0) & 3) * 256, 16384);
                if (i == 3 + X_D - 1) { VMCNT(8 * X_D); __builtin_amdgcn_s_barrier(); OPAQUE(tag); }
            }
            unsigned pubd = npre + 12 - X_D;
            prog = lds_min3(Lb + CTL + C_G);
            if (prog < KV_START) {
#pragma unroll
                for (int d = X_D - 1; d >= 0; --d) { VMCNT(8 * d); lds_st(fulla, npre + 12 - d); }
                pubd = npre + 12;
                gate(KV_START);
            }
            STAMP(1);
#pragma unroll 1
            for (int c = 0; c < 32; ++c) {
                const bool isk = c < 5 || (c < 27 && !(c & 1));
                const int tile = c < 5 ? c : (c >= 27 ? c - 16 : (isk ? (c + 4) >> 1 : (c - 5) >> 1));
                LAS char* sl = L + ((c & 8) ? S_BASE : P_BASE) + (c & 7) * SLOT;
                VMCNT(8 * (KV_D - 1)); lds_st(fulla, max(pubd, (unsigned)(12 + c - KV_D + 1)));
                gate(c + 1);
                dma_chunk<KV_AUX>(isk ? rk : rv, sl, vo256, ((tile + trot) & 15) * 8192, 4096);
            }
            STAMP(2);
            {
                int ps = (lane & 3) + rotp; ps = ps >= 3 ? ps - 3 : ps;
                const int pw = b * 32 + xcd * 4 + ((hl + 1 + ps) & 3);
                GAS u64* fp = (GAS u64*)(ws + WS_F1 + (pw * 3 + min((lane >> 2) & 3, 2)) * 128);
#pragma unroll
                for (int d = KV_D - 1; d >= POLL_AT; --d) { VMCNT(8 * d); lds_st(fulla, 12 + 32 - d); }
                u64 f = __hip_atomic_load(fp, RLX_AGENT);
#pragma unroll
                for (int d = POLL_AT - 1; d >= 0; --d) { VMCNT(8 * d + 1); lds_st(fulla, 12 + 32 - d); }
                STAMP(3);
                unsigned m = 0;
                for (unsigned sp = 0;; ++sp) {
                    unsigned r = (unsigned)__builtin_amdgcn_ballot_w64(f == tag) & 0x777u;
                    if (sp >= SPIN_CAP) r = 0x777u;
                    if (r != m) { m = r; lds_st(Lb + CTL + C_RDY, m); }
                    if (m == 0x777u) break;
                    f = __hip_atomic_load(fp, RLX_AGENT);
                }
            }
            STAMP(4);
        } else {
            const __amdgpu_buffer_rsrc_t rwq = mkrsrc(wq, 512 * 8192);
            int vo8k[4];
#pragma unroll
            for (int k = 0; k < 4; ++k) vo8k[k] = (4 * k + rq) * 8192 + 16 * (pos ^ (rq << 2) ^ k) - 1024 * k;
#pragma unroll
            for (int n = 2; n < 16; ++n) {
                if (n >= 2 + W_D) { VMCNT(8 * (W_D - 1)); lds_st(fulla, n - W_D + 1); }
                if (n == 10) gate(XS1 + 2);
                if (n == 13) gate(XS2 + 2);
                dma_chunk<0>(rwq, L + (n & 8 ? S_BASE : P_BASE) + (n & 7) * SLOT, vo8k, wq_off(n), 131072);
                if (n == 3) { VMCNT(16); __builtin_amdgcn_s_barrier(); }
            }
            STAMP(1);
#pragma unroll
            for (int d = W_D - 1; d >= 0; --d) { VMCNT(8 * d); lds_st(fulla, 16 - d); }
            int vow[4];
#pragma unroll
            for (int k = 0; k < 4; ++k) vow[k] = (4 * k + rq) * 1024 + 16 * ((pos ^ (rq << 1)) & 7) + 512 * (pos >> 3) - 1024 * k;
#pragma unroll 1
            for (int i = 0; i < 16; ++i) {
                const int ci = i < 3 ? 8 + i : (i < 11 ? i - 3 : i);
                gate(i < 3 ? WO_START : 30 + i);
                dma_chunk<0>(rwo, L + ci * SLOT, vow, wo_off(ci), 16384);
                VMCNT(8 * (WO_D - 1)); if (i + 2 > WO_D) lds_st(fulla, 16 + i + 2 - WO_D);
            }
#pragma unroll
            for (int d = WO_D - 2; d >= 0; --d) { VMCNT(8 * d); lds_st(fulla, 32 - d); }
            STAMP(2);
            STAMP(3);
        }
    } else {
        const int t = w;
        if (tid < 64) lds_st(Lb + CTL + tid * 4, 0u);
        tag = __hip_atomic_load((GAS u64*)(ws + WS_EPOCH + b * 256), RLX_AGENT) + 1;
        const int r32 = lane & 31, hh = lane >> 5;
        volatile LAS unsigned* ctl = (volatile LAS unsigned*)(L + CTL);
        const int fr_ = ((r32 & 3) << 2) | ((r32 >> 2) & 3);
        unsigned rbx = Lb + t * SLOT + ((256 * r32 + 16 * (hh ^ fr_)) ^ (128 * (b & 1)));
        const int pr = (r32 & 19) | ((r32 & 4) << 1) | ((r32 & 8) >> 1);
        const int fp_ = ((pr & 3) << 2) | ((pr >> 2) & 3);
        unsigned rbk = Lb + P_BASE + 256 * pr + 16 * (hh ^ fp_);
        const int blk = (lane >> 4) & 1, q4 = (lane & 15) >> 2, p4 = lane & 3;
        unsigned tb0 = Lb + 256 * (8 * hh + q4) + 64 * q4 + 16 * ((2 * blk + (p4 & 1)) ^ (2 * hh)) + 8 * (p4 >> 1);
        unsigned availW = 2, availK = 0;
        auto waitW = [&](unsigned need) { if (__builtin_expect(availW < need, 0)) for (unsigned sp = 0; availW < need && sp < SPIN_CAP; ++sp) { if (sp) __builtin_amdgcn_s_sleep(CP_SLEEP); availW = rfl(ctl[C_FULLW / 4]); } CBAR(); };
        auto waitKs = [&](unsigned need) { if (__builtin_expect(availK < need, 0)) for (unsigned sp = 0; availK < need && sp < SPIN_CAP; ++sp) { if (sp) __builtin_amdgcn_s_sleep(CP_SLEEP); availK = rfl(ctl[C_FULLK / 4]); } CBAR(); };
        auto waitK = [&](unsigned n) { waitKs(12 + n); };
        f32x16 acc[4];
#pragma unroll
        for (int d = 0; d < 4; ++d) acc[d] = f32x16{};
        {
            v4u fa[2][4], fb[2][4], xf[32];
            const unsigned tbw0 = tb0 + P_BASE, tbw1 = tb0 + S_BASE;
            asm volatile("s_waitcnt vmcnt(0) lgkmcnt(0)" ::: "memory");
            __builtin_amdgcn_s_barrier();
            CBAR();
            STAMP(14);
            xf[0] = row_rd(rbx + X_BASE); xf[1] = row_rd((rbx + X_BASE) ^ 32u);
            tr_chunk(tbw0, 0, fa);
#pragma unroll
            for (int ks = 2; ks < 8; ++ks) xf[ks] = row_rd(((rbx + X_BASE) ^ (unsigned)(32 * ks)));
            ctl[C_G / 4 + t] = 1;
#pragma unroll
            for (int n = 0; n < 16; ++n) {
                const int xq = n == XS1 ? 1 : (n == XS2 ? 2 : (n == XS3 ? 3 : 0));
                if (n & 1) waitW(n + 3 < 16 ? n + 3 : 16);
                if (xq) waitKs(npre + 3 * xq + t + 1);
                const int offn = ((n + 1) & 7) * SLOT;
                const unsigned tbw = n + 1 < 8 ? tbw0 : tbw1;
                const unsigned xn = rbx + (xq == 1 ? S_BASE + 2 * SLOT : (xq == 2 ? S_BASE + 5 * SLOT : X_BASE));
                v2u fw;
                SB();
#pragma unroll
                for (int g = 0; g < 8; ++g) {
                    if (n & 1) { acc[g >> 1] = mfma32(fb[g & 1][g >> 1], xf[2 * n + (g & 1)], acc[g >> 1]); if (n < 15) tr_frag_g(tbw, offn, g, fa); }
                    else { acc[g >> 1] = mfma32(fa[g & 1][g >> 1], xf[2 * n + (g & 1)], acc[g >> 1]); tr_frag_g(tbw, offn, g, fb); }
                    if (xq) xf[8 * xq + g] = row_rd(xn ^ (unsigned)(32 * g));
                    if (g == 3 && !(n & 1)) fw = *(volatile LAS v2u*)(L + CTL);
                    if (g == 0 && !(n & 1) && n > 0) ctl[C_G / 4 + t] = n + 1;
                    if (g == 7 && n == 14) ctl[C_G / 4 + t] = 16;
                    if (g == 7 && !(n & 1)) { availK = rfl(fw[0]); availW = max(2u, rfl(fw[1])); }
                    SB();
                }
            }
        }
        asm volatile("" :: "v"(tag));
        STAMP(1);
        constexpr float CS = 0.08838834764831845f * 1.4426950408889634f;
        constexpr float THR = 8.0f;
        v4u qf[8], kf[8];
        unsigned ka[8], va[8];
#pragma unroll
        for (int i = 0; i < 8; ++i) { ka[i] = rbk ^ (unsigned)(32 * i); va[i] = (tb0 + P_BASE) ^ (unsigned)(64 * (i >> 1) + 16 * (i & 1)); OPAQUE(ka[i]); OPAQUE(va[i]); }
        waitK(2);
#pragma unroll
        for (int ks = 0; ks < 8; ++ks) kf[ks] = row_rd(ka[ks]);
#pragma unroll
        for (int d = 0; d < 4; ++d) { acc[d] *= CS; qf[2 * d] = pack8(acc[d], 0); qf[2 * d + 1] = pack8(acc[d], 1); }
#pragma unroll
        for (int d = 0; d < 4; ++d) acc[d] = f32x16{};
        float lsum = 0.f;
        f32x16 sA, sB, negm; v4u vf[8], pe[4][2], cw;
        unsigned ctla = Lb + CTL + C_FULLK, pka = Lb + CTL + C_G + 4 * t, relv = 17;
        OPAQUE(ctla); OPAQUE(pka); OPAQUE(relv);
        auto decide = [&](float tm, f32x16& nxt, int dead) {
            if (__builtin_expect(__builtin_amdgcn_ballot_w64(tm > THR) != 0ull, 0)) {
                const float dl = fmaxf(swapmax(tm), 0.f), f = __builtin_amdgcn_exp2f(-dl);
                lsum *= f;
#pragma unroll
                for (int d = 0; d < 4; ++d) acc[d] *= f;
#pragma unroll
                for (int i = 0; i < 16; ++i) { nxt[i] -= dl; negm[i] -= dl; }
#pragma unroll
                for (int q = 0; q < 4; ++q) if (!(dead >> q & 1)) {
#pragma unroll
                    for (int i = 0; i < 8; ++i) { const unsigned u = pe[q][i >> 2][i & 3]; pe[q][i >> 2][i & 3] = cvtpk(__uint_as_float(u << 16) * f, __uint_as_float(u & 0xffff0000u) * f); }
                }
            }
        };
        auto block = [&](f32x16& cur, f32x16& nxt, int pw, int pr, int vslot, int kslot, unsigned rinc) -> float {
            float m = 0.f, m1 = 0.f, s0 = lsum, s1 = 0.f; unsigned fk = 0;
            SB();
#pragma unroll
            for (int g = 0; g < 16; ++g) {
                if (g < 8) nxt = mfma32(kf[g], qf[g], g == 0 ? negm : nxt);
                else acc[(g - 8) >> 1] = mfma32(vf[g - 8], pe[pr][g & 1], acc[(g - 8) >> 1]);
                if (g >= 3 && g < 11) { const int i = g - 3; vf[i] = tr2(va[2 * (i >> 1)], va[2 * (i >> 1) + 1], vslot * SLOT + (i & 1) * 4096); }
                if (g >= 11 && g < 15) { const int i = 2 * (g - 11); kf[i] = row_rd(ka[i] + kslot * SLOT); kf[i + 1] = row_rd(ka[i + 1] + kslot * SLOT); }
                const int e0 = g < 3 ? 2 * g : g + 3, e1 = g < 3 ? 2 * g + 2 : g + 4, p0 = g == 0 ? 0 : (g - 1 < 3 ? 2 * (g - 1) : g + 2);
#pragma unroll
                for (int e = e0; e < e1 && e < 16; ++e) cur[e] = __builtin_amdgcn_exp2f(cur[e]);
#pragma unroll
                for (int e = p0; e < e0 && e < 16; ++e) { if (e & 1) { s1 = fadd_s(s1, cur[e]); pe[pw][e >> 3][(e >> 1) & 3] = cvtpk(cur[e - 1], cur[e]); } else s0 = fadd_s(s0, cur[e]); }
                if (g == 11) { m = max3f(nxt[0], nxt[1], nxt[2]); m1 = max3f(nxt[3], nxt[4], nxt[5]); }
                if (g == 12) { m = max3f(m, nxt[6], nxt[7]); m1 = max3f(m1, nxt[8], nxt[9]); }
                if (g == 13) { m = max3f(m, nxt[10], nxt[11]); m1 = max3f(m1, nxt[12], nxt[13]); }
                if (g == 14) { m = max3f(m, nxt[14], nxt[15]); m = max3f(m, m1, m1); relv += rinc; *(volatile LAS unsigned*)(size_t)pka = relv; }
                if (g == FK_G) fk = *(volatile LAS unsigned*)(size_t)ctla;
                if (g == 15) { availK = rfl(fk); lsum = s0 + s1; OPAQUE(lsum); }
                SB();
            }
            return m;
        };
        auto estep = [&](f32x16& cur, f32x16& nxt, int pw, int kslot) -> float {
            float s0 = 0.f, s1 = 0.f;
            SB();
#pragma unroll
            for (int g = 0; g < 8; ++g) {
                nxt = mfma32(kf[g], qf[g], g == 0 ? negm : nxt);
                kf[g] = row_rd(ka[g] + kslot * SLOT);
                cur[2 * g] = __builtin_amdgcn_exp2f(cur[2 * g]); cur[2 * g + 1] = __builtin_amdgcn_exp2f(cur[2 * g + 1]);
                if (g > 0) { pe[pw][(g - 1) >> 2][(g - 1) & 3] = cvtpk(cur[2 * g - 2], cur[2 * g - 1]); s0 = fadd_s(s0, cur[2 * g - 2]); s1 = fadd_s(s1, cur[2 * g - 1]); }
                SB();
            }
            relv += 1; *(volatile LAS unsigned*)(size_t)pka = relv;
            pe[pw][1][3] = cvtpk(cur[14], cur[15]);
            lsum += (s0 + cur[14]) + (s1 + cur[15]);
            float m = fmaxf(nxt[0], nxt[1]);
#pragma unroll
            for (int i = 2; i < 16; i += 2) m = fmaxf(fmaxf(m, nxt[i]), nxt[i + 1]);
            return m;
        };
#pragma unroll
        for (int ks = 0; ks < 8; ++ks) { sA = mfma32(kf[ks], qf[ks], ks == 0 ? f32x16{} : sA); kf[ks] = row_rd(ka[ks] + SLOT); SB(); }
        relv += 1; *(volatile LAS unsigned*)(size_t)pka = relv;
        {
            float m = fmaxf(sA[0], sA[1]);
#pragma unroll
            for (int i = 2; i < 16; i += 2) m = fmaxf(fmaxf(m, sA[i]), sA[i + 1]);
            m = swapmax(m);
#pragma unroll
            for (int i = 0; i < 16; ++i) { sA[i] -= m; negm[i] = -m; }
            OPAQUE(negm);
        }
        STAMP(2);
        waitK(5);
        decide(estep(sA, sB, 0, 2), sB, 14);
        decide(estep(sB, sA, 1, 3), sA, 12);
        decide(estep(sA, sB, 2, 4), sB, 8);
#pragma unroll 1
        for (int j = 0; j < 12; j += 4) {
            waitK(2 * j + 7); decide(block(sB, sA, 3, 0, 5, 6, 2), sA, 1);
#pragma unroll
            for (int i = 0; i < 8; ++i) { ka[i] ^= S_BASE; OPAQUE(ka[i]); }
            waitK(2 * j + 9); decide(block(sA, sB, 0, 1, 7, 0, 2), sB, 2);
#pragma unroll
            for (int i = 0; i < 8; ++i) { va[i] ^= S_BASE; OPAQUE(va[i]); }
            waitK(2 * j + 11); decide(block(sB, sA, 1, 2, 1, 2, 2), sA, 4);
            waitK(j == 8 ? 28 : 2 * j + 13); decide(block(sA, sB, 2, 3, 3, 4, j == 8 ? 1 : 2), sB, 8);
        }
        {
            float s0 = 0.f, s1 = 0.f;
            cw = *(volatile LAS v4u*)(size_t)ctla;
#pragma unroll
            for (int g = 0; g < 8; ++g) {
                const float e0 = __builtin_amdgcn_exp2f(sB[2 * g]), e1 = __builtin_amdgcn_exp2f(sB[2 * g + 1]);
                s0 += e0; s1 += e1; pe[3][g >> 2][g & 3] = cvtpk(e0, e1);
            }
            lsum += s0 + s1;
            availK = rfl(cw[0]); availW = rfl(cw[1]);
        }
        STAMP(3);
        const int l16 = lane * 16;
        v4u pf[3][8];
        unsigned got = 0;
        auto rdy = [&]() -> unsigned { const unsigned r = (rfl(ctl[C_RDY / 4]) >> (4 * t)) & 7u & ~got; CBAR(); return r; };
        auto take = [&](int k) {
            got |= 1u << k;
            int ps = k + rotp; ps = ps >= 3 ? ps - 3 : ps;
            const int so = (int)WS_O + (b * 32 + xcd * 4 + ((hl + 1 + ps) & 3)) * 24576 + t * 8192;
#pragma unroll
            for (int i = 0; i < 8; ++i) pf[k][i] = __builtin_amdgcn_raw_buffer_load_b128(rws, l16 + (i & 3) * 1024, so + (i >> 2) * 4096, 16);
        };
        auto must = [&](int k) {
            unsigned r = rdy();
            if (!(got >> k & 1)) { for (unsigned sp = 0; !(r >> k & 1) && sp < 4 * SPIN_CAP; ++sp) r = rdy(); take(k); }
#pragma unroll
            for (int j = k + 1; j < 3; ++j) if (r >> j & 1) take(j);
        };
        float inv = __builtin_amdgcn_rcpf(swapsum(lsum));
        OPAQUE(inv);
        unsigned tbo = Lb + 256 * (8 * hh + q4) + 16 * ((8 * (p4 & 1) + blk) ^ (q4 << 1)) + 8 * (p4 >> 1);
        OPAQUE(tbo);
        v4u fa[2][4], fb[2][4];
        waitW(20);
        waitK(29);
        auto vfrag = [&](int slot, int g, v4u (&fr)[2][4]) { fr[g & 1][g >> 1] = tr2(va[2 * (g >> 1)], va[2 * (g >> 1) + 1], slot * SLOT + (g & 1) * 4096); };
#pragma unroll
        for (int g = 0; g < 8; ++g) vfrag(4, g, fa);
#pragma unroll
        for (int e = 0; e < 4; ++e) {
            if (e < 3) waitK(30 + e);
            unsigned fk = 0;
            SB();
#pragma unroll
            for (int g = 0; g < 8; ++g) {
                if (e & 1) { acc[g >> 1] = mfma32(fb[g & 1][g >> 1], pe[e][g & 1], acc[g >> 1]); if (e < 3) vfrag(5 + e, g, fa); else tro_frag_g(tbo, 0, g, fa); }
                else { acc[g >> 1] = mfma32(fa[g & 1][g >> 1], pe[e][g & 1], acc[g >> 1]); vfrag(5 + e, g, fb); }
                if (g == 0 && e == 0) { relv += 1; *(volatile LAS unsigned*)(size_t)pka = relv; }
                if (g == 4 && e < 2) fk = *(volatile LAS unsigned*)(size_t)ctla;
                if (g == 2 && e == 3) cw = *(volatile LAS v4u*)(size_t)ctla;
                if (g == 7 && e < 2) availK = rfl(fk);
                SB();
            }
        }
        STAMP(4);
        availW = rfl(cw[1]);
        CBAR();
        v4u cur[8];
        {
            const __amdgpu_buffer_rsrc_t ro = mkrsrc(ws + WS_O + wgid * 24576 + t * 8192, 8192);
#pragma unroll
            for (int d = 0; d < 4; ++d) {
                acc[d] *= inv; cur[2 * d] = pack8(acc[d], 0); cur[2 * d + 1] = pack8(acc[d], 1);
                __builtin_amdgcn_raw_buffer_store_b128(cur[2 * d], ro, l16 + (2 * d) * 1024, 0, 16);
                __builtin_amdgcn_raw_buffer_store_b128(cur[2 * d + 1], ro, l16 + (2 * d + 1) * 1024, 0, 16);
            }
        }
#pragma unroll
        for (int d = 0; d < 4; ++d) acc[d] = f32x16{};
        STAMP(5);
        auto gemm4 = [&](unsigned tb, const v4u* cur, auto FIRST, auto LAST) {
            OPAQUE(tb);
            SB();
            gemm_step<true>(acc, fa, cur[0], cur[1], tb, SLOT, fb);
            gemm_step<true>(acc, fb, cur[2], cur[3], tb, 2 * SLOT, fa);
            if (FIRST) {
                VMCNT(0);
                if (lane == 0) __hip_atomic_store((GAS u64*)(ws + WS_F1 + (wgid * 3 + t) * 128), tag, RLX_AGENT);
                { const unsigned r = rdy(); if (r & 1) take(0); if (r & 2) take(1); if (TAKE3 && (r & 4)) take(2); }
                SB();
            }
            gemm_step<true>(acc, fa, cur[4], cur[5], tb, 3 * SLOT, fb);
            if (!LAST) gemm_step<true>(acc, fb, cur[6], cur[7], tb, 4 * SLOT, fa);
            else gemm_step<false>(acc, fb, cur[6], cur[7], 0, 0, fa);
        };
        {
            constexpr std::integral_constant<bool, true> T{};
            constexpr std::integral_constant<bool, false> F{};
            waitW(24);
            gemm4(tbo, cur, T, F);
            STAMP(6);
            must(0); waitW(27);
            STAMP(11);
            gemm4(tbo + 4 * SLOT, pf[0], F, F);
            must(1); waitW(29);
            STAMP(12);
            gemm4(tbo + 8 * SLOT, pf[1], F, F);
            must(2); waitW(32);
            STAMP(13);
            gemm4(tbo + 12 * SLOT, pf[2], F, T);
        }
        STAMP(7);
        {
            const __amdgpu_buffer_rsrc_t rp = mkrsrc(ws + WS_P + wgid * 24576 + t * 8192, 8192);
#pragma unroll
            for (int d = 0; d < 4; ++d) {
                __builtin_amdgcn_raw_buffer_store_b128(pack8(acc[d], 0), rp, l16 + (2 * d) * 1024, 0, 16);
                __builtin_amdgcn_raw_buffer_store_b128(pack8(acc[d], 1), rp, l16 + (2 * d + 1) * 1024, 0, 16);
            }
        }
        ctl[C_DONE / 4] = 1;
        VMCNT(0);
        if (lane < 2) __hip_atomic_store((GAS u64*)(ws + WS_F2 + ((wgid * 3 + t) * 2 + lane) * 128), tag, RLX_AGENT);
        STAMP(8);
    }
    if (xcd < 6 && w < 4) {
        const int t = xcd >> 1, f = 4 * (xcd & 1) + w;
        if (w == 3) {
            for (unsigned sp = 0; rfl(lds_ld(Lb + CTL + C_DONE)) == 0u && sp < SPIN_CAP; ++sp) __builtin_amdgcn_s_sleep(2);
            GAS u64* fp = (GAS u64*)(ws + WS_F2 + (((b * 32 + (lane & 7) * 4 + hl) * 3 + t) * 2 + (xcd & 1)) * 128);
            for (unsigned sp = 0; sp < SPIN_CAP; ++sp) {
                const u64 fl = __hip_atomic_load(fp, RLX_AGENT);
                if (__builtin_amdgcn_ballot_w64(fl != tag) == 0ull) break;
            }
            lds_st(Lb + CTL + C_READY, 1);
            if (xcd == 0 && hl == 0 && lane == 0) __hip_atomic_store((GAS u64*)(ws + WS_EPOCH + b * 256), tag, RLX_AGENT);
        } else {
            for (unsigned sp = 0; rfl(lds_ld(Lb + CTL + C_READY)) == 0u && sp < SPIN_CAP; ++sp) {}
        }
        CBAR();
        STAMP(9);
        v4u pp[8];
#pragma unroll
        for (int xx = 0; xx < 8; ++xx) pp[xx] = __builtin_amdgcn_raw_buffer_load_b128(rws, lane * 16, (int)WS_P + (b * 32 + xx * 4 + hl) * 24576 + t * 8192 + f * 1024, 16);
        float s[8];
#pragma unroll
        for (int k = 0; k < 8; ++k) s[k] = 0.f;
#pragma unroll
        for (int xx = 0; xx < 8; ++xx)
#pragma unroll
            for (int k = 0; k < 4; ++k) { s[2 * k] += __uint_as_float(pp[xx][k] << 16); s[2 * k + 1] += __uint_as_float(pp[xx][k] & 0xffff0000u); }
        v4u o = {cvtpk(s[0], s[1]), cvtpk(s[2], s[3]), cvtpk(s[4], s[5]), cvtpk(s[6], s[7])};
        *(v4u*)((char*)out + ((size_t)(b * 96 + 32 * t + (lane & 31)) * 512 + 64 * hl + 256 * (lane >> 5) + 8 * f) * 2) = o;
        STAMP(10);
    }
    STAMP_FLUSH(ws);
    asm volatile(".Lfk_end:");
}

extern "C" void kernel_launch(void* const* d_in, const int* in_sizes, int n_in, void* d_out, int out_size, void* d_ws, size_t ws_size, hipStream_t stream) {
    (void)hipFuncSetAttribute((const void*)fused_kernel, hipFuncAttributeMaxDynamicSharedMemorySize, LDS_BYTES);
    fused_kernel<<<256, 384, LDS_BYTES, stream>>>(d_in[0], d_in[1], d_in[2], d_in[3], d_in[4], d_out, (char*)d_ws);
}
```

```cpp
#define TLB2 1
#include <hip/hip_runtime.h>
#include <hip/hip_bf16.h>
#include <type_traits>

typedef unsigned v4u __attribute__((ext_vector_type(4)));
typedef unsigned v2u __attribute__((ext_vector_type(2)));
typedef float f32x16 __attribute__((ext_vector_type(16)));
typedef float f32x2 __attribute__((ext_vector_type(2)));
typedef __bf16 bf16x8 __attribute__((ext_vector_type(8)));
typedef __bf16 bf16x2 __attribute__((ext_vector_type(2)));
typedef short s16x4 __attribute__((ext_vector_type(4)));
typedef unsigned long long u64;
#define LAS __attribute__((address_space(3)))
#define GAS __attribute__((address_space(1)))
#define DI __device__ __forceinline__
#define RLX_AGENT __ATOMIC_RELAXED, __HIP_MEMORY_SCOPE_AGENT

#define KV_AUX 2
#define KV_D 5
#define W_D 3
#define CLR_SLEEP 4
#define LD_SLEEP 1
#define CP_SLEEP 1
#define KV_PRE 0
#define X_D 3
#define LD_PRIO 3
#define TROT idx
#define F1_EARLY 1
#define P_SLEEP 0
#define CODE_PF 1
#define FK_G 10
#define POLL_AT 2
#define RDY_FRESH 1
#define TAKE3 1
#define W_AUX 0
#define WO_AUX 2
#define WO_PF 0
#define XS1 1
#define XS2 3
#define XS3 9
#define TYPEB 0
#define WO_START 24
#define WO_D 5
#define KV_START 1

#define STAMP(i)
#define STAMP_DECL
#define STAMP_FLUSH(ws)

constexpr int SLOT = 8192;
constexpr int P_BASE = 0, X_BASE = 8 * SLOT, S_BASE = 11 * SLOT, CTL = 19 * SLOT;
constexpr int LDS_BYTES = CTL + 512;
constexpr int C_FULLK = 0, C_FULLW = 4, C_G = 16  , C_READY = 64, C_DONE = 68, C_RDY = 8;
constexpr size_t WS_EPOCH = 0;
constexpr size_t WS_F1 = 4096;
constexpr size_t WS_F2 = WS_F1 + 768 * 128;
constexpr size_t WS_O = WS_F2 + 1536 * 128;
constexpr size_t WS_P = WS_O + 256 * 24576;
constexpr int WS_BYTES = (int)(WS_P + 256 * 24576);
constexpr unsigned SPIN_CAP = 1u << 22;

DI f32x16 mfma32(v4u a, v4u b, f32x16 c) {
    return __builtin_amdgcn_mfma_f32_32x32x16_bf16(__builtin_bit_cast(bf16x8, a), __builtin_bit_cast(bf16x8, b), c, 0, 0, 0);
}
DI unsigned cvtpk(float lo, float hi) { f32x2 v = {lo, hi}; bf16x2 b = __builtin_convertvector(v, bf16x2); return __builtin_bit_cast(unsigned, b); }
DI __amdgpu_buffer_rsrc_t mkrsrc(const void* p, int bytes) { return __builtin_amdgcn_make_buffer_rsrc((void*)p, 0, bytes, 0x00020000); }
DI unsigned rfl(unsigned x) { return __builtin_amdgcn_readfirstlane(x); }
DI v4u pack8(const f32x16& a, int s) {
    return v4u{cvtpk(a[8 * s], a[8 * s + 1]), cvtpk(a[8 * s + 2], a[8 * s + 3]), cvtpk(a[8 * s + 4], a[8 * s + 5]), cvtpk(a[8 * s + 6], a[8 * s + 7])};
}
DI float swapmax(float x) { auto r = __builtin_amdgcn_permlane32_swap(__float_as_uint(x), __float_as_uint(x), false, false); return fmaxf(__uint_as_float(r[0]), __uint_as_float(r[1])); }
DI float swapsum(float x) { auto r = __builtin_amdgcn_permlane32_swap(__float_as_uint(x), __float_as_uint(x), false, false); return __uint_as_float(r[0]) + __uint_as_float(r[1]); }
DI float max3f(float a, float b, float c) { float r; asm("v_max3_f32 %0, %1, %2, %3" : "=v"(r) : "v"(a), "v"(b), "v"(c)); return r; }
DI float fadd_s(float a, float b) { float r; asm("v_add_f32_e32 %0, %1, %2" : "=v"(r) : "v"(a), "v"(b)); return r; }
#define SB() __builtin_amdgcn_sched_barrier(0)
#define OPAQUE(x) asm volatile("" : "+v"(x))
#define CBAR() asm volatile("" ::: "memory")
#define VMCNT(n) asm volatile("s_waitcnt vmcnt(%0)" :: "i"(n) : "memory")
#define LGKM0() asm volatile("s_waitcnt lgkmcnt(0)" ::: "memory")

DI unsigned lds_ld(unsigned addr) { unsigned r; asm volatile("ds_read_b32 %0, %1\n\ts_waitcnt lgkmcnt(0)" : "=v"(r) : "v"(addr) : "memory"); return r; }
DI void lds_st(unsigned addr, unsigned v) { asm volatile("ds_write_b32 %0, %1" :: "v"(addr), "v"(v) : "memory"); }
DI unsigned lds_min3(unsigned addr) {
    unsigned a, b, c;
    asm volatile("ds_read_b32 %0, %3\n\tds_read_b32 %1, %3 offset:4\n\tds_read_b32 %2, %3 offset:8\n\ts_waitcnt lgkmcnt(0)" : "=&v"(a), "=&v"(b), "=&v"(c) : "v"(addr) : "memory");
    return rfl(min(a, min(b, c)));
}

template <int AUX> DI void dma_chunk(__amdgpu_buffer_rsrc_t rs, LAS char* slot, const int (&voff)[4], int soff, int half) {
    __builtin_amdgcn_raw_ptr_buffer_load_lds(rs, (LAS void*)slot, 16, voff[0], soff, 0, AUX);
    __builtin_amdgcn_raw_ptr_buffer_load_lds(rs, (LAS void*)slot, 16, voff[1], soff, 1024, AUX);
    __builtin_amdgcn_raw_ptr_buffer_load_lds(rs, (LAS void*)slot, 16, voff[2], soff, 2048, AUX);
    __builtin_amdgcn_raw_ptr_buffer_load_lds(rs, (LAS void*)slot, 16, voff[3], soff, 3072, AUX);
    __builtin_amdgcn_raw_ptr_buffer_load_lds(rs, (LAS void*)(slot + 4096), 16, voff[0], soff + half, 0, AUX);
    __builtin_amdgcn_raw_ptr_buffer_load_lds(rs, (LAS void*)(slot + 4096), 16, voff[1], soff + half, 1024, AUX);
    __builtin_amdgcn_raw_ptr_buffer_load_lds(rs, (LAS void*)(slot + 4096), 16, voff[2], soff + half, 2048, AUX);
    __builtin_amdgcn_raw_ptr_buffer_load_lds(rs, (LAS void*)(slot + 4096), 16, voff[3], soff + half, 3072, AUX);
}
DI v2u tr_rd(unsigned addr, int off) { return __builtin_bit_cast(v2u, __builtin_amdgcn_ds_read_tr16_b64_v4i16((LAS s16x4*)(size_t)(addr + off))); }
DI v4u row_rd(unsigned addr) { return *(LAS v4u*)(size_t)addr; }
DI v4u tr_frag(unsigned tb, int off, int s, int ct) {
    v2u l2 = tr_rd(tb ^ (unsigned)(64 * ct), off + s * 4096), h2 = tr_rd(tb ^ (unsigned)(64 * ct + 16), off + s * 4096 + 1024);
    return v4u{l2[0], l2[1], h2[0], h2[1]};
}
DI void tr_frag_g(unsigned tb, int off, int g, v4u (&fr)[2][4]) { fr[g & 1][g >> 1] = tr_frag(tb, off, g & 1, g >> 1); }
DI void tr_chunk(unsigned tb, int off, v4u (&fr)[2][4]) {
#pragma unroll
    for (int g = 0; g < 8; ++g) tr_frag_g(tb, off, g, fr);
}
DI v4u tr2(unsigned a0, unsigned a1, int off) { v2u l2 = tr_rd(a0, off), h2 = tr_rd(a1, off + 1024); return v4u{l2[0], l2[1], h2[0], h2[1]}; }
DI void tro_frag_g(unsigned tb, int off, int g, v4u (&fr)[2][4]) {
    const unsigned a = tb ^ (unsigned)(32 * (g >> 1));
    v2u l2 = tr_rd(a, off + (g & 1) * 4096), h2 = tr_rd(a, off + (g & 1) * 4096 + 1024);
    fr[g & 1][g >> 1] = v4u{l2[0], l2[1], h2[0], h2[1]};
}
template <bool NEXT> DI void gemm_step(f32x16 (&acc)[4], const v4u (&cur)[2][4], v4u b0, v4u b1, unsigned tb, int off, v4u (&nxt)[2][4]) {
#pragma unroll
    for (int g = 0; g < 8; ++g) {
        acc[g >> 1] = mfma32(cur[g & 1][g >> 1], (g & 1) ? b1 : b0, acc[g >> 1]);
        if (NEXT) tro_frag_g(tb, off, g, nxt);
        SB();
    }
}

__global__ __launch_bounds__(384, 2) void fused_kernel(const void* __restrict__ x, const void* __restrict__ K, const void* __restrict__ V,
                                                      const void* __restrict__ wq, const void* __restrict__ wo, void* __restrict__ out, char* ws) {
    extern __shared__ __attribute__((aligned(16))) char smem[];
    LAS char* L = (LAS char*)smem;
    const unsigned Lb = (unsigned)(size_t)L;
    const int tid = threadIdx.x, lane = tid & 63, w = rfl(tid >> 6);
    const int xcd = blockIdx.x & 7, idx = blockIdx.x >> 3;
    const int hl = idx & 3, b = idx >> 2, h = xcd * 4 + hl, bh = b * 32 + h;
    const int wgid = bh;
    STAMP_DECL
    STAMP(0);
    const __amdgpu_buffer_rsrc_t rws = mkrsrc(ws, WS_BYTES);
    const int cb0 = ((b >> 1) + hl) & 3, rotp = b % 3;
    u64 tag = 0;
    constexpr int npre = 0;
    const int pos = lane & 15, rq = lane >> 4;
    auto wq_off = [&](int n) -> int { return ((((n >> 2) + cb0) & 3) * 128 + ((n & 3) ^ (2 * (b & 1))) * 32) * 8192 + h * 256; };
    if (w == 5) {
        const char* p0 = (const char*)K + (size_t)bh * 131072; const char* p1 = (const char*)V + (size_t)bh * 131072;
        const char* p2 = (const char*)wo + (size_t)xcd * 524288; const char* p3 = ws + WS_O + (size_t)wgid * 24576;
        const char* p4 = ws + WS_P + (size_t)wgid * 24576; const char* p5 = (const char*)out + (size_t)b * 98304;
        unsigned j;
        asm volatile("s_load_dword %0, %1, 0\n\ts_load_dword %0, %2, 0\n\ts_load_dword %0, %3, 0\n\ts_load_dword %0, %4, 0\n\ts_load_dword %0, %5, 0\n\ts_load_dword %0, %6, 0\n\ts_waitcnt lgkmcnt(0)"
                     : "=&s"(j) : "s"(p0), "s"(p1), "s"(p2), "s"(p3), "s"(p4), "s"(p5) : "memory");
        return;
    }
    {
        unsigned long long pc; int len;
        asm volatile("s_getpc_b64 %0\n.Lfk_begin:\n\ts_mov_b32 %1, .Lfk_end-.Lfk_begin" : "=s"(pc), "=s"(len));
        __builtin_amdgcn_raw_ptr_buffer_load_lds(mkrsrc((const void*)pc, len), (LAS void*)(L + CTL + 256), 4, lane * 4, (idx * 5 + w) * 256, 0, 0);
    }
    {
        const bool isx = w < 3;
        const int sh = isx ? 10 : 13;
        const __amdgpu_buffer_rsrc_t r0 = mkrsrc(isx ? (const char*)x + (size_t)b * 98304 : (const char*)wq, isx ? 98304 : 512 * 8192);
        int vo[4];
#pragma unroll
        for (int k = 0; k < 4; ++k) vo[k] = ((4 * k + rq) << sh) + 16 * (pos ^ (rq << 2) ^ k) - 1024 * k;
        dma_chunk<W_AUX>(r0, L + (isx ? X_BASE + w * SLOT : P_BASE + (w - 3) * SLOT), vo, isx ? w * 32768 + cb0 * 256 : wq_off(w - 3), 16 << sh);
    }

    if (w >= 3) {
        int vo1k[4];
#pragma unroll
        for (int k = 0; k < 4; ++k) vo1k[k] = (4 * k + rq) * 1024 + 16 * (pos ^ (rq << 2) ^ k) - 1024 * k;
        const __amdgpu_buffer_rsrc_t rwo = mkrsrc(wo, 4096 * 1024);
        auto wo_off = [&](int ci) -> int {
            int head = hl;
            if (ci >= 4) { int ps = (ci >> 2) - 1 + rotp; ps = ps >= 3 ? ps - 3 : ps; head = (hl + 1 + ps) & 3; }
            return ((xcd * 4 + head) * 128 + (ci & 3) * 32) * 1024 + hl * 128;
        };
        __builtin_amdgcn_s_setprio(LD_PRIO);
        unsigned prog = 0;
        unsigned fulla = Lb + CTL + (w == 3 ? C_FULLK : C_FULLW);
        OPAQUE(fulla);
        auto gate = [&](unsigned need) {
            if (__builtin_expect(prog < need, 0)) {
                for (unsigned sp = 0; prog < need && sp < SPIN_CAP; ++sp) { if (sp) __builtin_amdgcn_s_sleep(LD_SLEEP); prog = lds_min3(Lb + CTL + C_G); }
            }
        };
        if (w == 3) {
            const __amdgpu_buffer_rsrc_t rx = mkrsrc((const char*)x + (size_t)b * 98304, 98304);
            const __amdgpu_buffer_rsrc_t rk = mkrsrc((const char*)K + (size_t)bh * 131072, 131072);
            const __amdgpu_buffer_rsrc_t rv = mkrsrc((const char*)V + (size_t)bh * 131072, 131072);
            int vo256[4];
#pragma unroll
            for (int k = 0; k < 4; ++k) vo256[k] = rq * 256 + 16 * (pos ^ (rq << 2) ^ k);
            const int trot = TROT;
            tag = __hip_atomic_load((GAS u64*)(ws + WS_EPOCH + b * 256), RLX_AGENT) + 1;
#pragma unroll
            for (int i = 3; i < 12; ++i) {
                const int cbi = i / 3, r = i % 3;
                if (i >= 3 + X_D) { VMCNT(8 * (X_D - 1)); lds_st(fulla, npre + i - X_D + 1); }
                if (i == 9) gate(1);
                dma_chunk<W_AUX>(rx, L + (cbi == 1 ? S_BASE + 2 * SLOT : (cbi == 2 ? S_BASE + 5 * SLOT : X_BASE)) + r * SLOT, vo1k, r * 32768 + ((cbi + cb0) & 3) * 256, 16384);
                if (i == 3 + X_D - 1) { VMCNT(8 * X_D); __builtin_amdgcn_s_barrier(); OPAQUE(tag); }
            }
            unsigned pubd = npre + 12 - X_D;
            prog = lds_min3(Lb + CTL + C_G);
            if (prog < KV_START) {
#pragma unroll
                for (int d = X_D - 1; d >= 0; --d) { VMCNT(8 * d); lds_st(fulla, npre + 12 - d); }
                pubd = npre + 12;
                gate(KV_START);
            }
            STAMP(1);
#pragma unroll 1
            for (int c = 0; c < 32; ++c) {
                const bool isk = c < 5 || (c < 27 && !(c & 1));
                const int tile = c < 5 ? c : (c >= 27 ? c - 16 : (isk ? (c + 4) >> 1 : (c - 5) >> 1));
                LAS char* sl = L + ((c & 8) ? S_BASE : P_BASE) + (c & 7) * SLOT;
                VMCNT(8 * (KV_D - 1)); lds_st(fulla, max(pubd, (unsigned)(12 + c - KV_D + 1)));
                gate(c + 1);
                dma_chunk<KV_AUX>(isk ? rk : rv, sl, vo256, ((tile + trot) & 15) * 8192, 4096);
            }
            STAMP(2);
            {
                int ps = (lane & 3) + rotp; ps = ps >= 3 ? ps - 3 : ps;
                const int pw = b * 32 + xcd * 4 + ((hl + 1 + ps) & 3);
                GAS u64* fp = (GAS u64*)(ws + WS_F1 + (pw * 3 + min((lane >> 2) & 3, 2)) * 128);
#pragma unroll
                for (int d = KV_D - 1; d >= POLL_AT; --d) { VMCNT(8 * d); lds_st(fulla, 12 + 32 - d); }
                u64 f = __hip_atomic_load(fp, RLX_AGENT);
#pragma unroll
                for (int d = POLL_AT - 1; d >= 0; --d) { VMCNT(8 * d + 1); lds_st(fulla, 12 + 32 - d); }
                STAMP(3);
                unsigned m = 0;
                for (unsigned sp = 0;; ++sp) {
                    unsigned r = (unsigned)__builtin_amdgcn_ballot_w64(f == tag) & 0x777u;
                    if (sp >= SPIN_CAP) r = 0x777u;
                    if (r != m) { m = r; lds_st(Lb + CTL + C_RDY, m); }
                    if (m == 0x777u) break;
                    f = __hip_atomic_load(fp, RLX_AGENT);
                }
            }
            STAMP(4);
        } else {
            const __amdgpu_buffer_rsrc_t rwq = mkrsrc(wq, 512 * 8192);
            int vo8k[4];
#pragma unroll
            for (int k = 0; k < 4; ++k) vo8k[k] = (4 * k + rq) * 8192 + 16 * (pos ^ (rq << 2) ^ k) - 1024 * k;
#pragma unroll
            for (int n = 2; n < 16; ++n) {
                if (n >= 2 + W_D) { VMCNT(8 * (W_D - 1)); lds_st(fulla, n - W_D + 1); }
                if (n == 10) gate(XS1 + 2);
                if (n == 13) gate(XS2 + 2);
                dma_chunk<W_AUX>(rwq, L + (n & 8 ? S_BASE : P_BASE) + (n & 7) * SLOT, vo8k, wq_off(n), 131072);
                if (n == 3) { VMCNT(16); __builtin_amdgcn_s_barrier(); }
            }
            STAMP(1);
#pragma unroll
            for (int d = W_D - 1; d >= 0; --d) { VMCNT(8 * d); lds_st(fulla, 16 - d); }
            int vow[4];
#pragma unroll
            for (int k = 0; k < 4; ++k) vow[k] = (4 * k + rq) * 1024 + 16 * ((pos ^ (rq << 1)) & 7) + 512 * (pos >> 3) - 1024 * k;
#pragma unroll 1
            for (int i = 0; i < 16; ++i) {
                const int ci = i < 3 ? 8 + i : (i < 11 ? i - 3 : i);
                gate(i < 3 ? WO_START : 30 + i);
                dma_chunk<WO_AUX>(rwo, L + ci * SLOT, vow, wo_off(ci), 16384);
                VMCNT(8 * (WO_D - 1)); if (i + 2 > WO_D) lds_st(fulla, 16 + i + 2 - WO_D);
            }
#pragma unroll
            for (int d = WO_D - 2; d >= 0; --d) { VMCNT(8 * d); lds_st(fulla, 32 - d); }
            STAMP(2);
            STAMP(3);
        }
    } else {
        const int t = w;
        if (tid < 64) lds_st(Lb + CTL + tid * 4, 0u);
        tag = __hip_atomic_load((GAS u64*)(ws + WS_EPOCH + b * 256), RLX_AGENT) + 1;
        const int r32 = lane & 31, hh = lane >> 5;
        volatile LAS unsigned* ctl = (volatile LAS unsigned*)(L + CTL);
        const int fr_ = ((r32 & 3) << 2) | ((r32 >> 2) & 3);
        unsigned rbx = Lb + t * SLOT + ((256 * r32 + 16 * (hh ^ fr_)) ^ (128 * (b & 1)));
        const int pr = (r32 & 19) | ((r32 & 4) << 1) | ((r32 & 8) >> 1);
        const int fp_ = ((pr & 3) << 2) | ((pr >> 2) & 3);
        unsigned rbk = Lb + P_BASE + 256 * pr + 16 * (hh ^ fp_);
        const int blk = (lane >> 4) & 1, q4 = (lane & 15) >> 2, p4 = lane & 3;
        unsigned tb0 = Lb + 256 * (8 * hh + q4) + 64 * q4 + 16 * ((2 * blk + (p4 & 1)) ^ (2 * hh)) + 8 * (p4 >> 1);
        unsigned availW = 2, availK = 0;
        auto waitW = [&](unsigned need) { if (__builtin_expect(availW < need, 0)) for (unsigned sp = 0; availW < need && sp < SPIN_CAP; ++sp) { if (sp) __builtin_amdgcn_s_sleep(CP_SLEEP); availW = rfl(ctl[C_FULLW / 4]); } CBAR(); };
        auto waitKs = [&](unsigned need) { if (__builtin_expect(availK < need, 0)) for (unsigned sp = 0; availK < need && sp < SPIN_CAP; ++sp) { if (sp) __builtin_amdgcn_s_sleep(CP_SLEEP); availK = rfl(ctl[C_FULLK / 4]); } CBAR(); };
        auto waitK = [&](unsigned n) { waitKs(12 + n); };
        f32x16 acc[4];
#pragma unroll
        for (int d = 0; d < 4; ++d) acc[d] = f32x16{};
        {
            v4u fa[2][4], fb[2][4], xf[32];
            const unsigned tbw0 = tb0 + P_BASE, tbw1 = tb0 + S_BASE;
            asm volatile("s_waitcnt vmcnt(0) lgkmcnt(0)" ::: "memory");
            __builtin_amdgcn_s_barrier();
            CBAR();
            STAMP(14);
            xf[0] = row_rd(rbx + X_BASE); xf[1] = row_rd((rbx + X_BASE) ^ 32u);
            tr_chunk(tbw0, 0, fa);
#pragma unroll
            for (int ks = 2; ks < 8; ++ks) xf[ks] = row_rd(((rbx + X_BASE) ^ (unsigned)(32 * ks)));
            ctl[C_G / 4 + t] = 1;
#pragma unroll
            for (int n = 0; n < 16; ++n) {
                const int xq = n == XS1 ? 1 : (n == XS2 ? 2 : (n == XS3 ? 3 : 0));
                if (n & 1) waitW(n + 3 < 16 ? n + 3 : 16);
                if (xq) waitKs(npre + 3 * xq + t + 1);
                const int offn = ((n + 1) & 7) * SLOT;
                const unsigned tbw = n + 1 < 8 ? tbw0 : tbw1;
                const unsigned xn = rbx + (xq == 1 ? S_BASE + 2 * SLOT : (xq == 2 ? S_BASE + 5 * SLOT : X_BASE));
                v2u fw;
                SB();
#pragma unroll
                for (int g = 0; g < 8; ++g) {
                    if (n & 1) { acc[g >> 1] = mfma32(fb[g & 1][g >> 1], xf[2 * n + (g & 1)], acc[g >> 1]); if (n < 15) tr_frag_g(tbw, offn, g, fa); }
                    else { acc[g >> 1] = mfma32(fa[g & 1][g >> 1], xf[2 * n + (g & 1)], acc[g >> 1]); tr_frag_g(tbw, offn, g, fb); }
                    if (xq) xf[8 * xq + g] = row_rd(xn ^ (unsigned)(32 * g));
                    if (g == 3 && !(n & 1)) fw = *(volatile LAS v2u*)(L + CTL);
                    if (g == 0 && !(n & 1) && n > 0) ctl[C_G / 4 + t] = n + 1;
                    if (g == 7 && n == 14) ctl[C_G / 4 + t] = 16;
                    if (g == 7 && !(n & 1)) { availK = rfl(fw[0]); availW = max(2u, rfl(fw[1])); }
                    SB();
                }
            }
        }
        asm volatile("" :: "v"(tag));
        STAMP(1);
        constexpr float CS = 0.08838834764831845f * 1.4426950408889634f;
        constexpr float THR = 8.0f;
        v4u qf[8], kf[8];
        unsigned ka[8], va[8];
#pragma unroll
        for (int i = 0; i < 8; ++i) { ka[i] = rbk ^ (unsigned)(32 * i); va[i] = (tb0 + P_BASE) ^ (unsigned)(64 * (i >> 1) + 16 * (i & 1)); OPAQUE(ka[i]); OPAQUE(va[i]); }
        waitK(2);
#pragma unroll
        for (int ks = 0; ks < 8; ++ks) kf[ks] = row_rd(ka[ks]);
#pragma unroll
        for (int d = 0; d < 4; ++d) { acc[d] *= CS; qf[2 * d] = pack8(acc[d], 0); qf[2 * d + 1] = pack8(acc[d], 1); }
#pragma unroll
        for (int d = 0; d < 4; ++d) acc[d] = f32x16{};
        float lsum = 0.f;
        f32x16 sA, sB, negm; v4u vf[8], pe[4][2], cw;
        unsigned ctla = Lb + CTL + C_FULLK, pka = Lb + CTL + C_G + 4 * t, relv = 17;
        OPAQUE(ctla); OPAQUE(pka); OPAQUE(relv);
        auto decide = [&](float tm, f32x16& nxt, int dead) {
            if (__builtin_expect(__builtin_amdgcn_ballot_w64(tm > THR) != 0ull, 0)) {
                const float dl = fmaxf(swapmax(tm), 0.f), f = __builtin_amdgcn_exp2f(-dl);
                lsum *= f;
#pragma unroll
                for (int d = 0; d < 4; ++d) acc[d] *= f;
#pragma unroll
                for (int i = 0; i < 16; ++i) { nxt[i] -= dl; negm[i] -= dl; }
#pragma unroll
                for (int q = 0; q < 4; ++q) if (!(dead >> q & 1)) {
#pragma unroll
                    for (int i = 0; i < 8; ++i) { const unsigned u = pe[q][i >> 2][i & 3]; pe[q][i >> 2][i & 3] = cvtpk(__uint_as_float(u << 16) * f, __uint_as_float(u & 0xffff0000u) * f); }
                }
            }
        };
        auto block = [&](f32x16& cur, f32x16& nxt, int pw, int pr, int vslot, int kslot, unsigned rinc) -> float {
            float m = 0.f, m1 = 0.f, s0 = lsum, s1 = 0.f; unsigned fk = 0;
            SB();
#pragma unroll
            for (int g = 0; g < 16; ++g) {
                if (g < 8) nxt = mfma32(kf[g], qf[g], g == 0 ? negm : nxt);
                else acc[(g - 8) >> 1] = mfma32(vf[g - 8], pe[pr][g & 1], acc[(g - 8) >> 1]);
                if (g >= 3 && g < 11) { const int i = g - 3; vf[i] = tr2(va[2 * (i >> 1)], va[2 * (i >> 1) + 1], vslot * SLOT + (i & 1) * 4096); }
                if (g >= 11 && g < 15) { const int i = 2 * (g - 11); kf[i] = row_rd(ka[i] + kslot * SLOT); kf[i + 1] = row_rd(ka[i + 1] + kslot * SLOT); }
                const int e0 = g < 3 ? 2 * g : g + 3, e1 = g < 3 ? 2 * g + 2 : g + 4, p0 = g == 0 ? 0 : (g - 1 < 3 ? 2 * (g - 1) : g + 2);
#pragma unroll
                for (int e = e0; e < e1 && e < 16; ++e) cur[e] = __builtin_amdgcn_exp2f(cur[e]);
#pragma unroll
                for (int e = p0; e < e0 && e < 16; ++e) { if (e & 1) { s1 = fadd_s(s1, cur[e]); pe[pw][e >> 3][(e >> 1) & 3] = cvtpk(cur[e - 1], cur[e]); } else s0 = fadd_s(s0, cur[e]); }
                if (g == 11) { m = max3f(nxt[0], nxt[1], nxt[2]); m1 = max3f(nxt[3], nxt[4], nxt[5]); }
                if (g == 12) { m = max3f(m, nxt[6], nxt[7]); m1 = max3f(m1, nxt[8], nxt[9]); }
                if (g == 13) { m = max3f(m, nxt[10], nxt[11]); m1 = max3f(m1, nxt[12], nxt[13]); }
                if (g == 14) { m = max3f(m, nxt[14], nxt[15]); m = max3f(m, m1, m1); relv += rinc; *(volatile LAS unsigned*)(size_t)pka = relv; }
                if (g == FK_G) fk = *(volatile LAS unsigned*)(size_t)ctla;
                if (g == 15) { availK = rfl(fk); lsum = s0 + s1; OPAQUE(lsum); }
                SB();
            }
            return m;
        };
        auto estep = [&](f32x16& cur, f32x16& nxt, int pw, int kslot) -> float {
            float s0 = 0.f, s1 = 0.f;
            SB();
#pragma unroll
            for (int g = 0; g < 8; ++g) {
                nxt = mfma32(kf[g], qf[g], g == 0 ? negm : nxt);
                kf[g] = row_rd(ka[g] + kslot * SLOT);
                cur[2 * g] = __builtin_amdgcn_exp2f(cur[2 * g]); cur[2 * g + 1] = __builtin_amdgcn_exp2f(cur[2 * g + 1]);
                if (g > 0) { pe[pw][(g - 1) >> 2][(g - 1) & 3] = cvtpk(cur[2 * g - 2], cur[2 * g - 1]); s0 = fadd_s(s0, cur[2 * g - 2]); s1 = fadd_s(s1, cur[2 * g - 1]); }
                SB();
            }
            relv += 1; *(volatile LAS unsigned*)(size_t)pka = relv;
            pe[pw][1][3] = cvtpk(cur[14], cur[15]);
            lsum += (s0 + cur[14]) + (s1 + cur[15]);
            float m = fmaxf(nxt[0], nxt[1]);
#pragma unroll
            for (int i = 2; i < 16; i += 2) m = fmaxf(fmaxf(m, nxt[i]), nxt[i + 1]);
            return m;
        };
#pragma unroll
        for (int ks = 0; ks < 8; ++ks) { sA = mfma32(kf[ks], qf[ks], ks == 0 ? f32x16{} : sA); kf[ks] = row_rd(ka[ks] + SLOT); SB(); }
        relv += 1; *(volatile LAS unsigned*)(size_t)pka = relv;
        {
            float m = fmaxf(sA[0], sA[1]);
#pragma unroll
            for (int i = 2; i < 16; i += 2) m = fmaxf(fmaxf(m, sA[i]), sA[i + 1]);
            m = swapmax(m);
#pragma unroll
            for (int i = 0; i < 16; ++i) { sA[i] -= m; negm[i] = -m; }
            OPAQUE(negm);
        }
        STAMP(2);
        waitK(5);
        decide(estep(sA, sB, 0, 2), sB, 14);
        decide(estep(sB, sA, 1, 3), sA, 12);
        decide(estep(sA, sB, 2, 4), sB, 8);
#pragma unroll 1
        for (int j = 0; j < 12; j += 4) {
            waitK(2 * j + 7); decide(block(sB, sA, 3, 0, 5, 6, 2), sA, 1);
#pragma unroll
            for (int i = 0; i < 8; ++i) { ka[i] ^= S_BASE; OPAQUE(ka[i]); }
            waitK(2 * j + 9); decide(block(sA, sB, 0, 1, 7, 0, 2), sB, 2);
#pragma unroll
            for (int i = 0; i < 8; ++i) { va[i] ^= S_BASE; OPAQUE(va[i]); }
            waitK(2 * j + 11); decide(block(sB, sA, 1, 2, 1, 2, 2), sA, 4);
            waitK(j == 8 ? 28 : 2 * j + 13); decide(block(sA, sB, 2, 3, 3, 4, j == 8 ? 1 : 2), sB, 8);
        }
        {
            float s0 = 0.f, s1 = 0.f;
            cw = *(volatile LAS v4u*)(size_t)ctla;
#pragma unroll
            for (int g = 0; g < 8; ++g) {
                const float e0 = __builtin_amdgcn_exp2f(sB[2 * g]), e1 = __builtin_amdgcn_exp2f(sB[2 * g + 1]);
                s0 += e0; s1 += e1; pe[3][g >> 2][g & 3] = cvtpk(e0, e1);
            }
            lsum += s0 + s1;
            availK = rfl(cw[0]); availW = rfl(cw[1]);
        }
        STAMP(3);
        const int l16 = lane * 16;
        v4u pf[3][8];
        unsigned got = 0;
        auto rdy = [&]() -> unsigned { const unsigned r = (rfl(ctl[C_RDY / 4]) >> (4 * t)) & 7u & ~got; CBAR(); return r; };
        auto take = [&](int k) {
            got |= 1u << k;
            int ps = k + rotp; ps = ps >= 3 ? ps - 3 : ps;
            const int so = (int)WS_O + (b * 32 + xcd * 4 + ((hl + 1 + ps) & 3)) * 24576 + t * 8192;
#pragma unroll
            for (int i = 0; i < 8; ++i) pf[k][i] = __builtin_amdgcn_raw_buffer_load_b128(rws, l16 + (i & 3) * 1024, so + (i >> 2) * 4096, 16);
        };
        auto must = [&](int k) {
            unsigned r = rdy();
            if (!(got >> k & 1)) { for (unsigned sp = 0; !(r >> k & 1) && sp < 4 * SPIN_CAP; ++sp) r = rdy(); take(k); }
#pragma unroll
            for (int j = k + 1; j < 3; ++j) if (r >> j & 1) take(j);
        };
        float inv = __builtin_amdgcn_rcpf(swapsum(lsum));
        OPAQUE(inv);
        unsigned tbo = Lb + 256 * (8 * hh + q4) + 16 * ((8 * (p4 & 1) + blk) ^ (q4 << 1)) + 8 * (p4 >> 1);
        OPAQUE(tbo);
        v4u fa[2][4], fb[2][4];
        waitW(20);
        waitK(29);
        auto vfrag = [&](int slot, int g, v4u (&fr)[2][4]) { fr[g & 1][g >> 1] = tr2(va[2 * (g >> 1)], va[2 * (g >> 1) + 1], slot * SLOT + (g & 1) * 4096); };
#pragma unroll
        for (int g = 0; g < 8; ++g) vfrag(4, g, fa);
#pragma unroll
        for (int e = 0; e < 4; ++e) {
            if (e < 3) waitK(30 + e);
            unsigned fk = 0;
            SB();
#pragma unroll
            for (int g = 0; g < 8; ++g) {
                if (e & 1) { acc[g >> 1] = mfma32(fb[g & 1][g >> 1], pe[e][g & 1], acc[g >> 1]); if (e < 3) vfrag(5 + e, g, fa); else tro_frag_g(tbo, 0, g, fa); }
                else { acc[g >> 1] = mfma32(fa[g & 1][g >> 1], pe[e][g & 1], acc[g >> 1]); vfrag(5 + e, g, fb); }
                if (g == 0 && e == 0) { relv += 1; *(volatile LAS unsigned*)(size_t)pka = relv; }
                if (g == 4 && e < 2) fk = *(volatile LAS unsigned*)(size_t)ctla;
                if (g == 2 && e == 3) cw = *(volatile LAS v4u*)(size_t)ctla;
                if (g == 7 && e < 2) availK = rfl(fk);
                SB();
            }
        }
        STAMP(4);
        availW = rfl(cw[1]);
        CBAR();
        v4u cur[8];
        {
            const __amdgpu_buffer_rsrc_t ro = mkrsrc(ws + WS_O + wgid * 24576 + t * 8192, 8192);
#pragma unroll
            for (int d = 0; d < 4; ++d) {
                acc[d] *= inv; cur[2 * d] = pack8(acc[d], 0); cur[2 * d + 1] = pack8(acc[d], 1);
                __builtin_amdgcn_raw_buffer_store_b128(cur[2 * d], ro, l16 + (2 * d) * 1024, 0, 16);
                __builtin_amdgcn_raw_buffer_store_b128(cur[2 * d + 1], ro, l16 + (2 * d + 1) * 1024, 0, 16);
            }
        }
#pragma unroll
        for (int d = 0; d < 4; ++d) acc[d] = f32x16{};
        STAMP(5);
        auto gemm4 = [&](unsigned tb, const v4u* cur, auto FIRST, auto LAST) {
            OPAQUE(tb);
            SB();
            gemm_step<true>(acc, fa, cur[0], cur[1], tb, SLOT, fb);
            gemm_step<true>(acc, fb, cur[2], cur[3], tb, 2 * SLOT, fa);
            if (FIRST) {
                VMCNT(0);
                if (lane == 0) __hip_atomic_store((GAS u64*)(ws + WS_F1 + (wgid * 3 + t) * 128), tag, RLX_AGENT);
                { const unsigned r = rdy(); if (r & 1) take(0); if (r & 2) take(1); if (TAKE3 && (r & 4)) take(2); }
                SB();
            }
            gemm_step<true>(acc, fa, cur[4], cur[5], tb, 3 * SLOT, fb);
            if (!LAST) gemm_step<true>(acc, fb, cur[6], cur[7], tb, 4 * SLOT, fa);
            else gemm_step<false>(acc, fb, cur[6], cur[7], 0, 0, fa);
        };
        {
            constexpr std::integral_constant<bool, true> T{};
            constexpr std::integral_constant<bool, false> F{};
            waitW(24);
            gemm4(tbo, cur, T, F);
            STAMP(6);
            must(0); waitW(27);
            STAMP(11);
            gemm4(tbo + 4 * SLOT, pf[0], F, F);
            must(1); waitW(29);
            STAMP(12);
            gemm4(tbo + 8 * SLOT, pf[1], F, F);
            must(2); waitW(32);
            STAMP(13);
            gemm4(tbo + 12 * SLOT, pf[2], F, T);
        }
        STAMP(7);
        {
            const __amdgpu_buffer_rsrc_t rp = mkrsrc(ws + WS_P + wgid * 24576 + t * 8192, 8192);
#pragma unroll
            for (int d = 0; d < 4; ++d) {
                __builtin_amdgcn_raw_buffer_store_b128(pack8(acc[d], 0), rp, l16 + (2 * d) * 1024, 0, 16);
                __builtin_amdgcn_raw_buffer_store_b128(pack8(acc[d], 1), rp, l16 + (2 * d + 1) * 1024, 0, 16);
            }
        }
        ctl[C_DONE / 4] = 1;
        VMCNT(0);
        if (lane < 2) __hip_atomic_store((GAS u64*)(ws + WS_F2 + ((wgid * 3 + t) * 2 + lane) * 128), tag, RLX_AGENT);
        STAMP(8);
    }
    if (xcd < 6 && w < 4) {
        const int t = xcd >> 1, f = 4 * (xcd & 1) + w;
        if (w == 3) {
            for (unsigned sp = 0; rfl(lds_ld(Lb + CTL + C_DONE)) == 0u && sp < SPIN_CAP; ++sp) __builtin_amdgcn_s_sleep(2);
            GAS u64* fp = (GAS u64*)(ws + WS_F2 + (((b * 32 + (lane & 7) * 4 + hl) * 3 + t) * 2 + (xcd & 1)) * 128);
            for (unsigned sp = 0; sp < SPIN_CAP; ++sp) {
                const u64 fl = __hip_atomic_load(fp, RLX_AGENT);
                if (__builtin_amdgcn_ballot_w64(fl != tag) == 0ull) break;
            }
            lds_st(Lb + CTL + C_READY, 1);
            if (xcd == 0 && hl == 0 && lane == 0) __hip_atomic_store((GAS u64*)(ws + WS_EPOCH + b * 256), tag, RLX_AGENT);
        } else {
            for (unsigned sp = 0; rfl(lds_ld(Lb + CTL + C_READY)) == 0u && sp < SPIN_CAP; ++sp) {}
        }
        CBAR();
        STAMP(9);
        v4u pp[8];
#pragma unroll
        for (int xx = 0; xx < 8; ++xx) pp[xx] = __builtin_amdgcn_raw_buffer_load_b128(rws, lane * 16, (int)WS_P + (b * 32 + xx * 4 + hl) * 24576 + t * 8192 + f * 1024, 16);
        float s[8];
#pragma unroll
        for (int k = 0; k < 8; ++k) s[k] = 0.f;
#pragma unroll
        for (int xx = 0; xx < 8; ++xx)
#pragma unroll
            for (int k = 0; k < 4; ++k) { s[2 * k] += __uint_as_float(pp[xx][k] << 16); s[2 * k + 1] += __uint_as_float(pp[xx][k] & 0xffff0000u); }
        v4u o = {cvtpk(s[0], s[1]), cvtpk(s[2], s[3]), cvtpk(s[4], s[5]), cvtpk(s[6], s[7])};
        *(v4u*)((char*)out + ((size_t)(b * 96 + 32 * t + (lane & 31)) * 512 + 64 * hl + 256 * (lane >> 5) + 8 * f) * 2) = o;
        STAMP(10);
    }
    STAMP_FLUSH(ws);
    asm volatile(".Lfk_end:");
}

extern "C" void kernel_launch(void* const* d_in, const int* in_sizes, int n_in, void* d_out, int out_size, void* d_ws, size_t ws_size, hipStream_t stream) {
    (void)hipFuncSetAttribute((const void*)fused_kernel, hipFuncAttributeMaxDynamicSharedMemorySize, LDS_BYTES);
    fused_kernel<<<256, 384, LDS_BYTES, stream>>>(d_in[0], d_in[1], d_in[2], d_in[3], d_in[4], d_out, (char*)d_ws);
}
```

```cpp
#define TLB2 1
#include <hip/hip_runtime.h>
#include <hip/hip_bf16.h>
#include <type_traits>

typedef unsigned v4u __attribute__((ext_vector_type(4)));
typedef unsigned v2u __attribute__((ext_vector_type(2)));
typedef float f32x16 __attribute__((ext_vector_type(16)));
typedef float f32x2 __attribute__((ext_vector_type(2)));
typedef __bf16 bf16x8 __attribute__((ext_vector_type(8)));
typedef __bf16 bf16x2 __attribute__((ext_vector_type(2)));
typedef short s16x4 __attribute__((ext_vector_type(4)));
typedef unsigned long long u64;
#define LAS __attribute__((address_space(3)))
#define GAS __attribute__((address_space(1)))
#define DI __device__ __forceinline__
#define RLX_AGENT __ATOMIC_RELAXED, __HIP_MEMORY_SCOPE_AGENT

#define KV_AUX 2
#define KV_D 6
#define W_D 3
#define CLR_SLEEP 4
#define LD_SLEEP 1
#define CP_SLEEP 1
#define KV_PRE 0
#define X_D 3
#define LD_PRIO 3
#define TROT idx
#define F1_EARLY 1
#define P_SLEEP 0
#define CODE_PF 1
#define FK_G 10
#define POLL_AT 2
#define RDY_FRESH 1
#define TAKE3 1
#define PACE 300
#define PACE_OFF 450
#define PACE_ABS 1
#define NL 1
#define XS1 1
#define XS2 3
#define XS3 9
#define TYPEB 0
#define WO_START 24
#define WO_D 5
#define KV_START 1

#define STAMP(i)
#define STAMP_DECL
#define STAMP_FLUSH(ws)

constexpr int SLOT = 8192;
constexpr int P_BASE = 0, X_BASE = 8 * SLOT, S_BASE = 11 * SLOT, CTL = 19 * SLOT;
constexpr int LDS_BYTES = CTL + 512;
constexpr int C_FULLK = 0, C_FULLW = 4, C_G = 16  , C_READY = 64, C_DONE = 68, C_RDY = 8;
constexpr size_t WS_EPOCH = 0;
constexpr size_t WS_F1 = 4096;
constexpr size_t WS_F2 = WS_F1 + 768 * 128;
constexpr size_t WS_O = WS_F2 + 1536 * 128;
constexpr size_t WS_P = WS_O + 256 * 24576;
constexpr int WS_BYTES = (int)(WS_P + 256 * 24576);
constexpr unsigned SPIN_CAP = 1u << 22;

DI f32x16 mfma32(v4u a, v4u b, f32x16 c) {
    return __builtin_amdgcn_mfma_f32_32x32x16_bf16(__builtin_bit_cast(bf16x8, a), __builtin_bit_cast(bf16x8, b), c, 0, 0, 0);
}
DI unsigned cvtpk(float lo, float hi) { f32x2 v = {lo, hi}; bf16x2 b = __builtin_convertvector(v, bf16x2); return __builtin_bit_cast(unsigned, b); }
DI __amdgpu_buffer_rsrc_t mkrsrc(const void* p, int bytes) { return __builtin_amdgcn_make_buffer_rsrc((void*)p, 0, bytes, 0x00020000); }
DI unsigned rfl(unsigned x) { return __builtin_amdgcn_readfirstlane(x); }
DI v4u pack8(const f32x16& a, int s) {
    return v4u{cvtpk(a[8 * s], a[8 * s + 1]), cvtpk(a[8 * s + 2], a[8 * s + 3]), cvtpk(a[8 * s + 4], a[8 * s + 5]), cvtpk(a[8 * s + 6], a[8 * s + 7])};
}
DI float swapmax(float x) { auto r = __builtin_amdgcn_permlane32_swap(__float_as_uint(x), __float_as_uint(x), false, false); return fmaxf(__uint_as_float(r[0]), __uint_as_float(r[1])); }
DI float swapsum(float x) { auto r = __builtin_amdgcn_permlane32_swap(__float_as_uint(x), __float_as_uint(x), false, false); return __uint_as_float(r[0]) + __uint_as_float(r[1]); }
DI float max3f(float a, float b, float c) { float r; asm("v_max3_f32 %0, %1, %2, %3" : "=v"(r) : "v"(a), "v"(b), "v"(c)); return r; }
DI float fadd_s(float a, float b) { float r; asm("v_add_f32_e32 %0, %1, %2" : "=v"(r) : "v"(a), "v"(b)); return r; }
#define SB() __builtin_amdgcn_sched_barrier(0)
#define OPAQUE(x) asm volatile("" : "+v"(x))
#define CBAR() asm volatile("" ::: "memory")
#define VMCNT(n) asm volatile("s_waitcnt vmcnt(%0)" :: "i"(n) : "memory")
#define LGKM0() asm volatile("s_waitcnt lgkmcnt(0)" ::: "memory")

DI unsigned lds_ld(unsigned addr) { unsigned r; asm volatile("ds_read_b32 %0, %1\n\ts_waitcnt lgkmcnt(0)" : "=v"(r) : "v"(addr) : "memory"); return r; }
DI void lds_st(unsigned addr, unsigned v) { asm volatile("ds_write_b32 %0, %1" :: "v"(addr), "v"(v) : "memory"); }
DI unsigned lds_min3(unsigned addr) {
    unsigned a, b, c;
    asm volatile("ds_read_b32 %0, %3\n\tds_read_b32 %1, %3 offset:4\n\tds_read_b32 %2, %3 offset:8\n\ts_waitcnt lgkmcnt(0)" : "=&v"(a), "=&v"(b), "=&v"(c) : "v"(addr) : "memory");
    return rfl(min(a, min(b, c)));
}

template <int AUX> DI void dma_chunk(__amdgpu_buffer_rsrc_t rs, LAS char* slot, const int (&voff)[4], int soff, int half) {
    __builtin_amdgcn_raw_ptr_buffer_load_lds(rs, (LAS void*)slot, 16, voff[0], soff, 0, AUX);
    __builtin_amdgcn_raw_ptr_buffer_load_lds(rs, (LAS void*)slot, 16, voff[1], soff, 1024, AUX);
    __builtin_amdgcn_raw_ptr_buffer_load_lds(rs, (LAS void*)slot, 16, voff[2], soff, 2048, AUX);
    __builtin_amdgcn_raw_ptr_buffer_load_lds(rs, (LAS void*)slot, 16, voff[3], soff, 3072, AUX);
    __builtin_amdgcn_raw_ptr_buffer_load_lds(rs, (LAS void*)(slot + 4096), 16, voff[0], soff + half, 0, AUX);
    __builtin_amdgcn_raw_ptr_buffer_load_lds(rs, (LAS void*)(slot + 4096), 16, voff[1], soff + half, 1024, AUX);
    __builtin_amdgcn_raw_ptr_buffer_load_lds(rs, (LAS void*)(slot + 4096), 16, voff[2], soff + half, 2048, AUX);
    __builtin_amdgcn_raw_ptr_buffer_load_lds(rs, (LAS void*)(slot + 4096), 16, voff[3], soff + half, 3072, AUX);
}
DI v2u tr_rd(unsigned addr, int off) { return __builtin_bit_cast(v2u, __builtin_amdgcn_ds_read_tr16_b64_v4i16((LAS s16x4*)(size_t)(addr + off))); }
DI v4u row_rd(unsigned addr) { return *(LAS v4u*)(size_t)addr; }
DI v4u tr_frag(unsigned tb, int off, int s, int ct) {
    v2u l2 = tr_rd(tb ^ (unsigned)(64 * ct), off + s * 4096), h2 = tr_rd(tb ^ (unsigned)(64 * ct + 16), off + s * 4096 + 1024);
    return v4u{l2[0], l2[1], h2[0], h2[1]};
}
DI void tr_frag_g(unsigned tb, int off, int g, v4u (&fr)[2][4]) { fr[g & 1][g >> 1] = tr_frag(tb, off, g & 1, g >> 1); }
DI void tr_chunk(unsigned tb, int off, v4u (&fr)[2][4]) {
#pragma unroll
    for (int g = 0; g < 8; ++g) tr_frag_g(tb, off, g, fr);
}
DI v4u tr2(unsigned a0, unsigned a1, int off) { v2u l2 = tr_rd(a0, off), h2 = tr_rd(a1, off + 1024); return v4u{l2[0], l2[1], h2[0], h2[1]}; }
DI void tro_frag_g(unsigned tb, int off, int g, v4u (&fr)[2][4]) {
    const unsigned a = tb ^ (unsigned)(32 * (g >> 1));
    v2u l2 = tr_rd(a, off + (g & 1) * 4096), h2 = tr_rd(a, off + (g & 1) * 4096 + 1024);
    fr[g & 1][g >> 1] = v4u{l2[0], l2[1], h2[0], h2[1]};
}
template <bool NEXT> DI void gemm_step(f32x16 (&acc)[4], const v4u (&cur)[2][4], v4u b0, v4u b1, unsigned tb, int off, v4u (&nxt)[2][4]) {
#pragma unroll
    for (int g = 0; g < 8; ++g) {
        acc[g >> 1] = mfma32(cur[g & 1][g >> 1], (g & 1) ? b1 : b0, acc[g >> 1]);
        if (NEXT) tro_frag_g(tb, off, g, nxt);
        SB();
    }
}

__global__ __launch_bounds__(384, 2) void fused_kernel(const void* __restrict__ x, const void* __restrict__ K, const void* __restrict__ V,
                                                      const void* __restrict__ wq, const void* __restrict__ wo, void* __restrict__ out, char* ws) {
    extern __shared__ __attribute__((aligned(16))) char smem[];
    LAS char* L = (LAS char*)smem;
    const unsigned Lb = (unsigned)(size_t)L;
    const int tid = threadIdx.x, lane = tid & 63, w = rfl(tid >> 6);
    const int xcd = blockIdx.x & 7, idx = blockIdx.x >> 3;
    const int hl = idx & 3, b = idx >> 2, h = xcd * 4 + hl, bh = b * 32 + h;
    const int wgid = bh;
    const unsigned long long tstart = __builtin_amdgcn_s_memrealtime();
    STAMP_DECL
    STAMP(0);
    const __amdgpu_buffer_rsrc_t rws = mkrsrc(ws, WS_BYTES);
    const int cb0 = ((b >> 1) + hl) & 3, rotp = b % 3;
    u64 tag = 0;
    constexpr int npre = 0;
    const int pos = lane & 15, rq = lane >> 4;
    auto wq_off = [&](int n) -> int { return ((((n >> 2) + cb0) & 3) * 128 + ((n & 3) ^ (2 * (b & 1))) * 32) * 8192 + h * 256; };
    if (w == 5) {
        const char* p0 = (const char*)K + (size_t)bh * 131072; const char* p1 = (const char*)V + (size_t)bh * 131072;
        const char* p2 = (const char*)wo + (size_t)xcd * 524288; const char* p3 = ws + WS_O + (size_t)wgid * 24576;
        const char* p4 = ws + WS_P + (size_t)wgid * 24576; const char* p5 = (const char*)out + (size_t)b * 98304;
        unsigned j;
        asm volatile("s_load_dword %0, %1, 0\n\ts_load_dword %0, %2, 0\n\ts_load_dword %0, %3, 0\n\ts_load_dword %0, %4, 0\n\ts_load_dword %0, %5, 0\n\ts_load_dword %0, %6, 0\n\ts_waitcnt lgkmcnt(0)"
                     : "=&s"(j) : "s"(p0), "s"(p1), "s"(p2), "s"(p3), "s"(p4), "s"(p5) : "memory");
        return;
    }
    {
        unsigned long long pc; int len;
        asm volatile("s_getpc_b64 %0\n.Lfk_begin:\n\ts_mov_b32 %1, .Lfk_end-.Lfk_begin" : "=s"(pc), "=s"(len));
        __builtin_amdgcn_raw_ptr_buffer_load_lds(mkrsrc((const void*)pc, len), (LAS void*)(L + CTL + 256), 4, lane * 4, (idx * 5 + w) * 256, 0, 0);
    }
    {
        const bool isx = w < 3;
        const int sh = isx ? 10 : 13;
        const __amdgpu_buffer_rsrc_t r0 = mkrsrc(isx ? (const char*)x + (size_t)b * 98304 : (const char*)wq, isx ? 98304 : 512 * 8192);
        int vo[4];
#pragma unroll
        for (int k = 0; k < 4; ++k) vo[k] = ((4 * k + rq) << sh) + 16 * (pos ^ (rq << 2) ^ k) - 1024 * k;
        dma_chunk<0>(r0, L + (isx ? X_BASE + w * SLOT : P_BASE + (w - 3) * SLOT), vo, isx ? w * 32768 + cb0 * 256 : wq_off(w - 3), 16 << sh);
    }

    if (w >= 3) {
        int vo1k[4];
#pragma unroll
        for (int k = 0; k < 4; ++k) vo1k[k] = (4 * k + rq) * 1024 + 16 * (pos ^ (rq << 2) ^ k) - 1024 * k;
        const __amdgpu_buffer_rsrc_t rwo = mkrsrc(wo, 4096 * 1024);
        auto wo_off = [&](int ci) -> int {
            int head = hl;
            if (ci >= 4) { int ps = (ci >> 2) - 1 + rotp; ps = ps >= 3 ? ps - 3 : ps; head = (hl + 1 + ps) & 3; }
            return ((xcd * 4 + head) * 128 + (ci & 3) * 32) * 1024 + hl * 128;
        };
        __builtin_amdgcn_s_setprio(LD_PRIO);
        unsigned prog = 0;
        unsigned fulla = Lb + CTL + (w == 3 ? C_FULLK : C_FULLW);
        OPAQUE(fulla);
        auto gate = [&](unsigned need) {
            if (__builtin_expect(prog < need, 0)) {
                for (unsigned sp = 0; prog < need && sp < SPIN_CAP; ++sp) { if (sp) __builtin_amdgcn_s_sleep(LD_SLEEP); prog = lds_min3(Lb + CTL + C_G); }
            }
        };
        if (w == 3) {
            const __amdgpu_buffer_rsrc_t rx = mkrsrc((const char*)x + (size_t)b * 98304, 98304);
            const __amdgpu_buffer_rsrc_t rk = mkrsrc((const char*)K + (size_t)bh * 131072, 131072);
            const __amdgpu_buffer_rsrc_t rv = mkrsrc((const char*)V + (size_t)bh * 131072, 131072);
            int vo256[4];
#pragma unroll
            for (int k = 0; k < 4; ++k) vo256[k] = rq * 256 + 16 * (pos ^ (rq << 2) ^ k);
            const int trot = TROT;
            tag = __hip_atomic_load((GAS u64*)(ws + WS_EPOCH + b * 256), RLX_AGENT) + 1;
#pragma unroll
            for (int i = 3; i < 12; ++i) {
                const int cbi = i / 3, r = i % 3;
                if (i >= 3 + X_D) { VMCNT(8 * (X_D - 1)); lds_st(fulla, npre + i - X_D + 1); }
                if (i == 9) gate(1);
                dma_chunk<0>(rx, L + (cbi == 1 ? S_BASE + 2 * SLOT : (cbi == 2 ? S_BASE + 5 * SLOT : X_BASE)) + r * SLOT, vo1k, r * 32768 + ((cbi + cb0) & 3) * 256, 16384);
                if (i == 3 + X_D - 1) { VMCNT(8 * X_D); __builtin_amdgcn_s_barrier(); OPAQUE(tag); }
            }
            unsigned pubd = npre + 12 - X_D;
            prog = lds_min3(Lb + CTL + C_G);
            if (prog < KV_START) {
#pragma unroll
                for (int d = X_D - 1; d >= 0; --d) { VMCNT(8 * d); lds_st(fulla, npre + 12 - d); }
                pubd = npre + 12;
                gate(KV_START);
            }
            STAMP(1);
            const unsigned long long tk0 = PACE_ABS ? tstart : __builtin_amdgcn_s_memrealtime();
#pragma unroll 1
            for (int c = 0; c < 32; ++c) {
                const bool isk = c < 5 || (c < 27 && !(c & 1));
                const int tile = c < 5 ? c : (c >= 27 ? c - 16 : (isk ? (c + 4) >> 1 : (c - 5) >> 1));
                LAS char* sl = L + ((c & 8) ? S_BASE : P_BASE) + (c & 7) * SLOT;
                VMCNT(8 * (KV_D - 1)); lds_st(fulla, max(pubd, (unsigned)(12 + c - KV_D + 1)));
                gate(c + 1);
                if (c >= 8) { const unsigned lim = PACE_OFF + (unsigned)(c - KV_D) * PACE / 10; for (unsigned sp = 0; (unsigned)(__builtin_amdgcn_s_memrealtime() - tk0) < lim && sp < 4096; ++sp) __builtin_amdgcn_s_sleep(1); }
                dma_chunk<KV_AUX>(isk ? rk : rv, sl, vo256, ((tile + trot) & 15) * 8192, 4096);
            }
            STAMP(2);
            {
                int ps = (lane & 3) + rotp; ps = ps >= 3 ? ps - 3 : ps;
                const int pw = b * 32 + xcd * 4 + ((hl + 1 + ps) & 3);
                GAS u64* fp = (GAS u64*)(ws + WS_F1 + (pw * 3 + min((lane >> 2) & 3, 2)) * 128);
#pragma unroll
                for (int d = KV_D - 1; d >= POLL_AT; --d) { VMCNT(8 * d); lds_st(fulla, 12 + 32 - d); }
                u64 f = __hip_atomic_load(fp, RLX_AGENT);
#pragma unroll
                for (int d = POLL_AT - 1; d >= 0; --d) { VMCNT(8 * d + 1); lds_st(fulla, 12 + 32 - d); }
                STAMP(3);
                unsigned m = 0;
                for (unsigned sp = 0;; ++sp) {
                    unsigned r = (unsigned)__builtin_amdgcn_ballot_w64(f == tag) & 0x777u;
                    if (sp >= SPIN_CAP) r = 0x777u;
                    if (r != m) { m = r; lds_st(Lb + CTL + C_RDY, m); }
                    if (m == 0x777u) break;
                    f = __hip_atomic_load(fp, RLX_AGENT);
                }
            }
            STAMP(4);
        } else {
            const __amdgpu_buffer_rsrc_t rwq = mkrsrc(wq, 512 * 8192);
            int vo8k[4];
#pragma unroll
            for (int k = 0; k < 4; ++k) vo8k[k] = (4 * k + rq) * 8192 + 16 * (pos ^ (rq << 2) ^ k) - 1024 * k;
#pragma unroll
            for (int n = 2; n < 16; ++n) {
                if (n >= 2 + W_D) { VMCNT(8 * (W_D - 1)); lds_st(fulla, n - W_D + 1); }
                if (n == 10) gate(XS1 + 2);
                if (n == 13) gate(XS2 + 2);
                dma_chunk<0>(rwq, L + (n & 8 ? S_BASE : P_BASE) + (n & 7) * SLOT, vo8k, wq_off(n), 131072);
                if (n == 3) { VMCNT(16); __builtin_amdgcn_s_barrier(); }
            }
            STAMP(1);
#pragma unroll
            for (int d = W_D - 1; d >= 0; --d) { VMCNT(8 * d); lds_st(fulla, 16 - d); }
            int vow[4];
#pragma unroll
            for (int k = 0; k < 4; ++k) vow[k] = (4 * k + rq) * 1024 + 16 * ((pos ^ (rq << 1)) & 7) + 512 * (pos >> 3) - 1024 * k;
#pragma unroll 1
            for (int i = 0; i < 16; ++i) {
                const int ci = i < 3 ? 8 + i : (i < 11 ? i - 3 : i);
                gate(i < 3 ? WO_START : 30 + i);
                dma_chunk<0>(rwo, L + ci * SLOT, vow, wo_off(ci), 16384);
                VMCNT(8 * (WO_D - 1)); if (i + 2 > WO_D) lds_st(fulla, 16 + i + 2 - WO_D);
            }
#pragma unroll
            for (int d = WO_D - 2; d >= 0; --d) { VMCNT(8 * d); lds_st(fulla, 32 - d); }
            STAMP(2);
            STAMP(3);
        }
    } else {
        const int t = w;
        if (tid < 64) lds_st(Lb + CTL + tid * 4, 0u);
        tag = __hip_atomic_load((GAS u64*)(ws + WS_EPOCH + b * 256), RLX_AGENT) + 1;
        const int r32 = lane & 31, hh = lane >> 5;
        volatile LAS unsigned* ctl = (volatile LAS unsigned*)(L + CTL);
        const int fr_ = ((r32 & 3) << 2) | ((r32 >> 2) & 3);
        unsigned rbx = Lb + t * SLOT + ((256 * r32 + 16 * (hh ^ fr_)) ^ (128 * (b & 1)));
        const int pr = (r32 & 19) | ((r32 & 4) << 1) | ((r32 & 8) >> 1);
        const int fp_ = ((pr & 3) << 2) | ((pr >> 2) & 3);
        unsigned rbk = Lb + P_BASE + 256 * pr + 16 * (hh ^ fp_);
        const int blk = (lane >> 4) & 1, q4 = (lane & 15) >> 2, p4 = lane & 3;
        unsigned tb0 = Lb + 256 * (8 * hh + q4) + 64 * q4 + 16 * ((2 * blk + (p4 & 1)) ^ (2 * hh)) + 8 * (p4 >> 1);
        unsigned availW = 2, availK = 0;
        auto waitW = [&](unsigned need) { if (__builtin_expect(availW < need, 0)) for (unsigned sp = 0; availW < need && sp < SPIN_CAP; ++sp) { if (sp) __builtin_amdgcn_s_sleep(CP_SLEEP); availW = rfl(ctl[C_FULLW / 4]); } CBAR(); };
        auto waitKs = [&](unsigned need) { if (__builtin_expect(availK < need, 0)) for (unsigned sp = 0; availK < need && sp < SPIN_CAP; ++sp) { if (sp) __builtin_amdgcn_s_sleep(CP_SLEEP); availK = rfl(ctl[C_FULLK / 4]); } CBAR(); };
        auto waitK = [&](unsigned n) { waitKs(12 + n); };
        f32x16 acc[4];
#pragma unroll
        for (int d = 0; d < 4; ++d) acc[d] = f32x16{};
        {
            v4u fa[2][4], fb[2][4], xf[32];
            const unsigned tbw0 = tb0 + P_BASE, tbw1 = tb0 + S_BASE;
            asm volatile("s_waitcnt vmcnt(0) lgkmcnt(0)" ::: "memory");
            __builtin_amdgcn_s_barrier();
            CBAR();
            STAMP(14);
            xf[0] = row_rd(rbx + X_BASE); xf[1] = row_rd((rbx + X_BASE) ^ 32u);
            tr_chunk(tbw0, 0, fa);
#pragma unroll
            for (int ks = 2; ks < 8; ++ks) xf[ks] = row_rd(((rbx + X_BASE) ^ (unsigned)(32 * ks)));
            ctl[C_G / 4 + t] = 1;
#pragma unroll
            for (int n = 0; n < 16; ++n) {
                const int xq = n == XS1 ? 1 : (n == XS2 ? 2 : (n == XS3 ? 3 : 0));
                if (n & 1) waitW(n + 3 < 16 ? n + 3 : 16);
                if (xq) waitKs(npre + 3 * xq + t + 1);
                const int offn = ((n + 1) & 7) * SLOT;
                const unsigned tbw = n + 1 < 8 ? tbw0 : tbw1;
                const unsigned xn = rbx + (xq == 1 ? S_BASE + 2 * SLOT : (xq == 2 ? S_BASE + 5 * SLOT : X_BASE));
                v2u fw;
                SB();
#pragma unroll
                for (int g = 0; g < 8; ++g) {
                    if (n & 1) { acc[g >> 1] = mfma32(fb[g & 1][g >> 1], xf[2 * n + (g & 1)], acc[g >> 1]); if (n < 15) tr_frag_g(tbw, offn, g, fa); }
                    else { acc[g >> 1] = mfma32(fa[g & 1][g >> 1], xf[2 * n + (g & 1)], acc[g >> 1]); tr_frag_g(tbw, offn, g, fb); }
                    if (xq) xf[8 * xq + g] = row_rd(xn ^ (unsigned)(32 * g));
                    if (g == 3 && !(n & 1)) fw = *(volatile LAS v2u*)(L + CTL);
                    if (g == 0 && !(n & 1) && n > 0) ctl[C_G / 4 + t] = n + 1;
                    if (g == 7 && n == 14) ctl[C_G / 4 + t] = 16;
                    if (g == 7 && !(n & 1)) { availK = rfl(fw[0]); availW = max(2u, rfl(fw[1])); }
                    SB();
                }
            }
        }
        asm volatile("" :: "v"(tag));
        STAMP(1);
        constexpr float CS = 0.08838834764831845f * 1.4426950408889634f;
        constexpr float THR = 8.0f;
        v4u qf[8], kf[8];
        unsigned ka[8], va[8];
#pragma unroll
        for (int i = 0; i < 8; ++i) { ka[i] = rbk ^ (unsigned)(32 * i); va[i] = (tb0 + P_BASE) ^ (unsigned)(64 * (i >> 1) + 16 * (i & 1)); OPAQUE(ka[i]); OPAQUE(va[i]); }
        waitK(2);
#pragma unroll
        for (int ks = 0; ks < 8; ++ks) kf[ks] = row_rd(ka[ks]);
#pragma unroll
        for (int d = 0; d < 4; ++d) { acc[d] *= CS; qf[2 * d] = pack8(acc[d], 0); qf[2 * d + 1] = pack8(acc[d], 1); }
#pragma unroll
        for (int d = 0; d < 4; ++d) acc[d] = f32x16{};
        float lsum = 0.f;
        f32x16 sA, sB, negm; v4u vf[8], pe[4][2], cw;
        unsigned ctla = Lb + CTL + C_FULLK, pka = Lb + CTL + C_G + 4 * t, relv = 17;
        OPAQUE(ctla); OPAQUE(pka); OPAQUE(relv);
        auto decide = [&](float tm, f32x16& nxt, int dead) {
            if (__builtin_expect(__builtin_amdgcn_ballot_w64(tm > THR) != 0ull, 0)) {
                const float dl = fmaxf(swapmax(tm), 0.f), f = __builtin_amdgcn_exp2f(-dl);
                lsum *= f;
#pragma unroll
                for (int d = 0; d < 4; ++d) acc[d] *= f;
#pragma unroll
                for (int i = 0; i < 16; ++i) { nxt[i] -= dl; negm[i] -= dl; }
#pragma unroll
                for (int q = 0; q < 4; ++q) if (!(dead >> q & 1)) {
#pragma unroll
                    for (int i = 0; i < 8; ++i) { const unsigned u = pe[q][i >> 2][i & 3]; pe[q][i >> 2][i & 3] = cvtpk(__uint_as_float(u << 16) * f, __uint_as_float(u & 0xffff0000u) * f); }
                }
            }
        };
        auto block = [&](f32x16& cur, f32x16& nxt, int pw, int pr, int vslot, int kslot, unsigned rinc) -> float {
            float m = 0.f, m1 = 0.f, s0 = lsum, s1 = 0.f; unsigned fk = 0;
            SB();
#pragma unroll
            for (int g = 0; g < 16; ++g) {
                if (g < 8) nxt = mfma32(kf[g], qf[g], g == 0 ? negm : nxt);
                else acc[(g - 8) >> 1] = mfma32(vf[g - 8], pe[pr][g & 1], acc[(g - 8) >> 1]);
                if (g >= 3 && g < 11) { const int i = g - 3; vf[i] = tr2(va[2 * (i >> 1)], va[2 * (i >> 1) + 1], vslot * SLOT + (i & 1) * 4096); }
                if (g >= 11 && g < 15) { const int i = 2 * (g - 11); kf[i] = row_rd(ka[i] + kslot * SLOT); kf[i + 1] = row_rd(ka[i + 1] + kslot * SLOT); }
                const int e0 = g < 3 ? 2 * g : g + 3, e1 = g < 3 ? 2 * g + 2 : g + 4, p0 = g == 0 ? 0 : (g - 1 < 3 ? 2 * (g - 1) : g + 2);
#pragma unroll
                for (int e = e0; e < e1 && e < 16; ++e) cur[e] = __builtin_amdgcn_exp2f(cur[e]);
#pragma unroll
                for (int e = p0; e < e0 && e < 16; ++e) { if (e & 1) { s1 = fadd_s(s1, cur[e]); pe[pw][e >> 3][(e >> 1) & 3] = cvtpk(cur[e - 1], cur[e]); } else s0 = fadd_s(s0, cur[e]); }
                if (g == 11) { m = max3f(nxt[0], nxt[1], nxt[2]); m1 = max3f(nxt[3], nxt[4], nxt[5]); }
                if (g == 12) { m = max3f(m, nxt[6], nxt[7]); m1 = max3f(m1, nxt[8], nxt[9]); }
                if (g == 13) { m = max3f(m, nxt[10], nxt[11]); m1 = max3f(m1, nxt[12], nxt[13]); }
                if (g == 14) { m = max3f(m, nxt[14], nxt[15]); m = max3f(m, m1, m1); relv += rinc; *(volatile LAS unsigned*)(size_t)pka = relv; }
                if (g == FK_G) fk = *(volatile LAS unsigned*)(size_t)ctla;
                if (g == 15) { availK = rfl(fk); lsum = s0 + s1; OPAQUE(lsum); }
                SB();
            }
            return m;
        };
        auto estep = [&](f32x16& cur, f32x16& nxt, int pw, int kslot) -> float {
            float s0 = 0.f, s1 = 0.f;
            SB();
#pragma unroll
            for (int g = 0; g < 8; ++g) {
                nxt = mfma32(kf[g], qf[g], g == 0 ? negm : nxt);
                kf[g] = row_rd(ka[g] + kslot * SLOT);
                cur[2 * g] = __builtin_amdgcn_exp2f(cur[2 * g]); cur[2 * g + 1] = __builtin_amdgcn_exp2f(cur[2 * g + 1]);
                if (g > 0) { pe[pw][(g - 1) >> 2][(g - 1) & 3] = cvtpk(cur[2 * g - 2], cur[2 * g - 1]); s0 = fadd_s(s0, cur[2 * g - 2]); s1 = fadd_s(s1, cur[2 * g - 1]); }
                SB();
            }
            relv += 1; *(volatile LAS unsigned*)(size_t)pka = relv;
            pe[pw][1][3] = cvtpk(cur[14], cur[15]);
            lsum += (s0 + cur[14]) + (s1 + cur[15]);
            float m = fmaxf(nxt[0], nxt[1]);
#pragma unroll
            for (int i = 2; i < 16; i += 2) m = fmaxf(fmaxf(m, nxt[i]), nxt[i + 1]);
            return m;
        };
#pragma unroll
        for (int ks = 0; ks < 8; ++ks) { sA = mfma32(kf[ks], qf[ks], ks == 0 ? f32x16{} : sA); kf[ks] = row_rd(ka[ks] + SLOT); SB(); }
        relv += 1; *(volatile LAS unsigned*)(size_t)pka = relv;
        {
            float m = fmaxf(sA[0], sA[1]);
#pragma unroll
            for (int i = 2; i < 16; i += 2) m = fmaxf(fmaxf(m, sA[i]), sA[i + 1]);
            m = swapmax(m);
#pragma unroll
            for (int i = 0; i < 16; ++i) { sA[i] -= m; negm[i] = -m; }
            OPAQUE(negm);
        }
        STAMP(2);
        waitK(5);
        decide(estep(sA, sB, 0, 2), sB, 14);
        decide(estep(sB, sA, 1, 3), sA, 12);
        decide(estep(sA, sB, 2, 4), sB, 8);
#pragma unroll 1
        for (int j = 0; j < 12; j += 4) {
            waitK(2 * j + 7); decide(block(sB, sA, 3, 0, 5, 6, 2), sA, 1);
#pragma unroll
            for (int i = 0; i < 8; ++i) { ka[i] ^= S_BASE; OPAQUE(ka[i]); }
            waitK(2 * j + 9); decide(block(sA, sB, 0, 1, 7, 0, 2), sB, 2);
#pragma unroll
            for (int i = 0; i < 8; ++i) { va[i] ^= S_BASE; OPAQUE(va[i]); }
            waitK(2 * j + 11); decide(block(sB, sA, 1, 2, 1, 2, 2), sA, 4);
            waitK(j == 8 ? 28 : 2 * j + 13); decide(block(sA, sB, 2, 3, 3, 4, j == 8 ? 1 : 2), sB, 8);
        }
        {
            float s0 = 0.f, s1 = 0.f;
            cw = *(volatile LAS v4u*)(size_t)ctla;
#pragma unroll
            for (int g = 0; g < 8; ++g) {
                const float e0 = __builtin_amdgcn_exp2f(sB[2 * g]), e1 = __builtin_amdgcn_exp2f(sB[2 * g + 1]);
                s0 += e0; s1 += e1; pe[3][g >> 2][g & 3] = cvtpk(e0, e1);
            }
            lsum += s0 + s1;
            availK = rfl(cw[0]); availW = rfl(cw[1]);
        }
        STAMP(3);
        const int l16 = lane * 16;
        v4u pf[3][8];
        unsigned got = 0;
        auto rdy = [&]() -> unsigned { const unsigned r = (rfl(ctl[C_RDY / 4]) >> (4 * t)) & 7u & ~got; CBAR(); return r; };
        auto take = [&](int k) {
            got |= 1u << k;
            int ps = k + rotp; ps = ps >= 3 ? ps - 3 : ps;
            const int so = (int)WS_O + (b * 32 + xcd * 4 + ((hl + 1 + ps) & 3)) * 24576 + t * 8192;
#pragma unroll
            for (int i = 0; i < 8; ++i) pf[k][i] = __builtin_amdgcn_raw_buffer_load_b128(rws, l16 + (i & 3) * 1024, so + (i >> 2) * 4096, 16);
        };
        auto must = [&](int k) {
            unsigned r = rdy();
            if (!(got >> k & 1)) { for (unsigned sp = 0; !(r >> k & 1) && sp < 4 * SPIN_CAP; ++sp) r = rdy(); take(k); }
#pragma unroll
            for (int j = k + 1; j < 3; ++j) if (r >> j & 1) take(j);
        };
        float inv = __builtin_amdgcn_rcpf(swapsum(lsum));
        OPAQUE(inv);
        unsigned tbo = Lb + 256 * (8 * hh + q4) + 16 * ((8 * (p4 & 1) + blk) ^ (q4 << 1)) + 8 * (p4 >> 1);
        OPAQUE(tbo);
        v4u fa[2][4], fb[2][4];
        waitW(20);
        waitK(29);
        auto vfrag = [&](int slot, int g, v4u (&fr)[2][4]) { fr[g & 1][g >> 1] = tr2(va[2 * (g >> 1)], va[2 * (g >> 1) + 1], slot * SLOT + (g & 1) * 4096); };
#pragma unroll
        for (int g = 0; g < 8; ++g) vfrag(4, g, fa);
#pragma unroll
        for (int e = 0; e < 4; ++e) {
            if (e < 3) waitK(30 + e);
            unsigned fk = 0;
            SB();
#pragma unroll
            for (int g = 0; g < 8; ++g) {
                if (e & 1) { acc[g >> 1] = mfma32(fb[g & 1][g >> 1], pe[e][g & 1], acc[g >> 1]); if (e < 3) vfrag(5 + e, g, fa); else tro_frag_g(tbo, 0, g, fa); }
                else { acc[g >> 1] = mfma32(fa[g & 1][g >> 1], pe[e][g & 1], acc[g >> 1]); vfrag(5 + e, g, fb); }
                if (g == 0 && e == 0) { relv += 1; *(volatile LAS unsigned*)(size_t)pka = relv; }
                if (g == 4 && e < 2) fk = *(volatile LAS unsigned*)(size_t)ctla;
                if (g == 2 && e == 3) cw = *(volatile LAS v4u*)(size_t)ctla;
                if (g == 7 && e < 2) availK = rfl(fk);
                SB();
            }
        }
        STAMP(4);
        availW = rfl(cw[1]);
        CBAR();
        v4u cur[8];
        {
            const __amdgpu_buffer_rsrc_t ro = mkrsrc(ws + WS_O + wgid * 24576 + t * 8192, 8192);
#pragma unroll
            for (int d = 0; d < 4; ++d) {
                acc[d] *= inv; cur[2 * d] = pack8(acc[d], 0); cur[2 * d + 1] = pack8(acc[d], 1);
                __builtin_amdgcn_raw_buffer_store_b128(cur[2 * d], ro, l16 + (2 * d) * 1024, 0, 16);
                __builtin_amdgcn_raw_buffer_store_b128(cur[2 * d + 1], ro, l16 + (2 * d + 1) * 1024, 0, 16);
            }
        }
#pragma unroll
        for (int d = 0; d < 4; ++d) acc[d] = f32x16{};
        STAMP(5);
        auto gemm4 = [&](unsigned tb, const v4u* cur, auto FIRST, auto LAST) {
            OPAQUE(tb);
            SB();
            gemm_step<true>(acc, fa, cur[0], cur[1], tb, SLOT, fb);
            gemm_step<true>(acc, fb, cur[2], cur[3], tb, 2 * SLOT, fa);
            if (FIRST) {
                VMCNT(0);
                if (lane == 0) __hip_atomic_store((GAS u64*)(ws + WS_F1 + (wgid * 3 + t) * 128), tag, RLX_AGENT);
                { const unsigned r = rdy(); if (r & 1) take(0); if (r & 2) take(1); if (TAKE3 && (r & 4)) take(2); }
                SB();
            }
            gemm_step<true>(acc, fa, cur[4], cur[5], tb, 3 * SLOT, fb);
            if (!LAST) gemm_step<true>(acc, fb, cur[6], cur[7], tb, 4 * SLOT, fa);
            else gemm_step<false>(acc, fb, cur[6], cur[7], 0, 0, fa);
        };
        {
            constexpr std::integral_constant<bool, true> T{};
            constexpr std::integral_constant<bool, false> F{};
            waitW(24);
            gemm4(tbo, cur, T, F);
            STAMP(6);
            must(0); waitW(27);
            STAMP(11);
            if (NL == 1) gemm4(tbo + 4 * SLOT, pf[0], F, F); else gemm4(tbo + 4 * SLOT, pf[0], F, T);
            if (NL == 1) {
                must(1); waitW(29);
                STAMP(12);
                gemm4(tbo + 8 * SLOT, pf[1], F, T);
            } else must(1);
            must(2); waitW(32);
            STAMP(13);
            const __amdgpu_buffer_rsrc_t rp = mkrsrc(ws + WS_P + wgid * 24576 + t * 8192, 8192);
            constexpr int KS = 8 * NL, M = 4 * KS;
            unsigned tb = tbo + (16 - 4 * NL) * SLOT;
            OPAQUE(tb);
            v4u fr[8], st0, st1;
            auto rd = [&](int i) { const int ct = i / KS, ks = i % KS; const unsigned ad = tb ^ (unsigned)(32 * ct); fr[i & 7] = tr2(ad, ad, ks * 4096); };
#pragma unroll
            for (int i = 0; i < 8; ++i) rd(i);
            SB();
#pragma unroll
            for (int i = 0; i < M; ++i) {
                const int ct = i / KS, ks = i % KS;
                acc[ct] = mfma32(fr[i & 7], pf[3 - NL + ks / 8][ks & 7], acc[ct]);
                if (i + 8 < M) rd(i + 8);
                if (ct > 0) {
                    if (ks == 2) st0 = pack8(acc[ct - 1], 0);
                    if (ks == 3) { st1 = pack8(acc[ct - 1], 1); __builtin_amdgcn_raw_buffer_store_b128(st0, rp, l16 + (2 * ct - 2) * 1024, 0, 16); }
                    if (ks == 4) __builtin_amdgcn_raw_buffer_store_b128(st1, rp, l16 + (2 * ct - 1) * 1024, 0, 16);
                }
                SB();
            }
            STAMP(7);
            __builtin_amdgcn_raw_buffer_store_b128(pack8(acc[3], 0), rp, l16 + 6 * 1024, 0, 16);
            __builtin_amdgcn_raw_buffer_store_b128(pack8(acc[3], 1), rp, l16 + 7 * 1024, 0, 16);
        }
        ctl[C_DONE / 4] = 1;
        VMCNT(0);
        if (lane < 2) __hip_atomic_store((GAS u64*)(ws + WS_F2 + ((wgid * 3 + t) * 2 + lane) * 128), tag, RLX_AGENT);
        STAMP(8);
    }
    if (xcd < 6 && w < 4) {
        const int t = xcd >> 1, f = 4 * (xcd & 1) + w;
        if (w == 3) {
            for (unsigned sp = 0; rfl(lds_ld(Lb + CTL + C_DONE)) == 0u && sp < SPIN_CAP; ++sp) __builtin_amdgcn_s_sleep(2);
            GAS u64* fp = (GAS u64*)(ws + WS_F2 + (((b * 32 + (lane & 7) * 4 + hl) * 3 + t) * 2 + (xcd & 1)) * 128);
            for (unsigned sp = 0; sp < SPIN_CAP; ++sp) {
                const u64 fl = __hip_atomic_load(fp, RLX_AGENT);
                if (__builtin_amdgcn_ballot_w64(fl != tag) == 0ull) break;
            }
            lds_st(Lb + CTL + C_READY, 1);
            if (xcd == 0 && hl == 0 && lane == 0) __hip_atomic_store((GAS u64*)(ws + WS_EPOCH + b * 256), tag, RLX_AGENT);
        } else {
            for (unsigned sp = 0; rfl(lds_ld(Lb + CTL + C_READY)) == 0u && sp < SPIN_CAP; ++sp) {}
        }
        CBAR();
        STAMP(9);
        v4u pp[8];
#pragma unroll
        for (int xx = 0; xx < 8; ++xx) pp[xx] = __builtin_amdgcn_raw_buffer_load_b128(rws, lane * 16, (int)WS_P + (b * 32 + xx * 4 + hl) * 24576 + t * 8192 + f * 1024, 16);
        float s[8];
#pragma unroll
        for (int k = 0; k < 8; ++k) s[k] = 0.f;
#pragma unroll
        for (int xx = 0; xx < 8; ++xx)
#pragma unroll
            for (int k = 0; k < 4; ++k) { s[2 * k] += __uint_as_float(pp[xx][k] << 16); s[2 * k + 1] += __uint_as_float(pp[xx][k] & 0xffff0000u); }
        v4u o = {cvtpk(s[0], s[1]), cvtpk(s[2], s[3]), cvtpk(s[4], s[5]), cvtpk(s[6], s[7])};
        *(v4u*)((char*)out + ((size_t)(b * 96 + 32 * t + (lane & 31)) * 512 + 64 * hl + 256 * (lane >> 5) + 8 * f) * 2) = o;
        STAMP(10);
    }
    STAMP_FLUSH(ws);
    asm volatile(".Lfk_end:");
}

extern "C" void kernel_launch(void* const* d_in, const int* in_sizes, int n_in, void* d_out, int out_size, void* d_ws, size_t ws_size, hipStream_t stream) {
    (void)hipFuncSetAttribute((const void*)fused_kernel, hipFuncAttributeMaxDynamicSharedMemorySize, LDS_BYTES);
    fused_kernel<<<256, 384, LDS_BYTES, stream>>>(d_in[0], d_in[1], d_in[2], d_in[3], d_in[4], d_out, (char*)d_ws);
}
```

```cpp
#include <hip/hip_runtime.h>
#include <hip/hip_bf16.h>
#include <type_traits>

typedef unsigned v4u __attribute__((ext_vector_type(4)));
typedef unsigned v2u __attribute__((ext_vector_type(2)));
typedef float f32x16 __attribute__((ext_vector_type(16)));
typedef float f32x2 __attribute__((ext_vector_type(2)));
typedef __bf16 bf16x8 __attribute__((ext_vector_type(8)));
typedef __bf16 bf16x2 __attribute__((ext_vector_type(2)));
typedef short s16x4 __attribute__((ext_vector_type(4)));
typedef unsigned long long u64;
#define LAS __attribute__((address_space(3)))
#define GAS __attribute__((address_space(1)))
#define DI __device__ __forceinline__
#define RLX_AGENT __ATOMIC_RELAXED, __HIP_MEMORY_SCOPE_AGENT

constexpr int KV_AUX = 2;
constexpr int KV_D = 5;
constexpr int W_D = 3;
constexpr int X_D = 3;
constexpr int WO_D = 5;
constexpr int XS1 = 1, XS2 = 3, XS3 = 9;
constexpr int WO_START = 24;
constexpr int WO_HOLD_I = 7, WO_HOLD_K = 39;

constexpr int SLOT = 8192;
constexpr int P_BASE = 0, X_BASE = 8 * SLOT, S_BASE = 11 * SLOT, CTL = 19 * SLOT;
constexpr int LDS_BYTES = CTL + 512;
constexpr int C_FULLK = 0, C_FULLW = 4, C_G = 16  , C_READY = 64, C_DONE = 68, C_RDY = 8;
constexpr size_t WS_EPOCH = 0;
constexpr size_t WS_F1 = 4096;
constexpr size_t WS_F2 = WS_F1 + 768 * 128;
constexpr size_t WS_O = WS_F2 + 1536 * 128;
constexpr size_t WS_P = WS_O + 256 * 24576;
constexpr int WS_BYTES = (int)(WS_P + 256 * 24576);
constexpr unsigned SPIN_CAP = 1u << 20;

DI f32x16 mfma32(v4u a, v4u b, f32x16 c) {
    return __builtin_amdgcn_mfma_f32_32x32x16_bf16(__builtin_bit_cast(bf16x8, a), __builtin_bit_cast(bf16x8, b), c, 0, 0, 0);
}
DI unsigned cvtpk(float lo, float hi) { f32x2 v = {lo, hi}; bf16x2 b = __builtin_convertvector(v, bf16x2); return __builtin_bit_cast(unsigned, b); }
DI __amdgpu_buffer_rsrc_t mkrsrc(const void* p, int bytes) { return __builtin_amdgcn_make_buffer_rsrc((void*)p, 0, bytes, 0x00020000); }
DI unsigned rfl(unsigned x) { return __builtin_amdgcn_readfirstlane(x); }
DI v4u pack8(const f32x16& a, int s) {
    return v4u{cvtpk(a[8 * s], a[8 * s + 1]), cvtpk(a[8 * s + 2], a[8 * s + 3]), cvtpk(a[8 * s + 4], a[8 * s + 5]), cvtpk(a[8 * s + 6], a[8 * s + 7])};
}
DI float swapmax(float x) { auto r = __builtin_amdgcn_permlane32_swap(__float_as_uint(x), __float_as_uint(x), false, false); return fmaxf(__uint_as_float(r[0]), __uint_as_float(r[1])); }
DI float swapsum(float x) { auto r = __builtin_amdgcn_permlane32_swap(__float_as_uint(x), __float_as_uint(x), false, false); return __uint_as_float(r[0]) + __uint_as_float(r[1]); }
DI float max3f(float a, float b, float c) { float r; asm("v_max3_f32 %0, %1, %2, %3" : "=v"(r) : "v"(a), "v"(b), "v"(c)); return r; }
DI float fadd_s(float a, float b) { float r; asm("v_add_f32_e32 %0, %1, %2" : "=v"(r) : "v"(a), "v"(b)); return r; }
#define SB() __builtin_amdgcn_sched_barrier(0)
#define OPAQUE(x) asm volatile("" : "+v"(x))
#define CBAR() asm volatile("" ::: "memory")
#define VMCNT(n) asm volatile("s_waitcnt vmcnt(%0)" :: "i"(n) : "memory")
#define LGKM0() asm volatile("s_waitcnt lgkmcnt(0)" ::: "memory")

DI unsigned lds_ld(unsigned addr) { unsigned r; asm volatile("ds_read_b32 %0, %1\n\ts_waitcnt lgkmcnt(0)" : "=v"(r) : "v"(addr) : "memory"); return r; }
DI void lds_st(unsigned addr, unsigned v) { asm volatile("ds_write_b32 %0, %1" :: "v"(addr), "v"(v) : "memory"); }
DI unsigned lds_min3(unsigned addr) {
    unsigned a, b, c;
    asm volatile("ds_read_b32 %0, %3\n\tds_read_b32 %1, %3 offset:4\n\tds_read_b32 %2, %3 offset:8\n\ts_waitcnt lgkmcnt(0)" : "=&v"(a), "=&v"(b), "=&v"(c) : "v"(addr) : "memory");
    return rfl(min(a, min(b, c)));
}

template <int AUX> DI void dma_chunk(__amdgpu_buffer_rsrc_t rs, LAS char* slot, const int (&voff)[4], int soff, int half) {
    __builtin_amdgcn_raw_ptr_buffer_load_lds(rs, (LAS void*)slot, 16, voff[0], soff, 0, AUX);
    __builtin_amdgcn_raw_ptr_buffer_load_lds(rs, (LAS void*)slot, 16, voff[1], soff, 1024, AUX);
    __builtin_amdgcn_raw_ptr_buffer_load_lds(rs, (LAS void*)slot, 16, voff[2], soff, 2048, AUX);
    __builtin_amdgcn_raw_ptr_buffer_load_lds(rs, (LAS void*)slot, 16, voff[3], soff, 3072, AUX);
    __builtin_amdgcn_raw_ptr_buffer_load_lds(rs, (LAS void*)(slot + 4096), 16, voff[0], soff + half, 0, AUX);
    __builtin_amdgcn_raw_ptr_buffer_load_lds(rs, (LAS void*)(slot + 4096), 16, voff[1], soff + half, 1024, AUX);
    __builtin_amdgcn_raw_ptr_buffer_load_lds(rs, (LAS void*)(slot + 4096), 16, voff[2], soff + half, 2048, AUX);
    __builtin_amdgcn_raw_ptr_buffer_load_lds(rs, (LAS void*)(slot + 4096), 16, voff[3], soff + half, 3072, AUX);
}
DI v2u tr_rd(unsigned addr, int off) { return __builtin_bit_cast(v2u, __builtin_amdgcn_ds_read_tr16_b64_v4i16((LAS s16x4*)(size_t)(addr + off))); }
DI v4u row_rd(unsigned addr) { return *(LAS v4u*)(size_t)addr; }
DI v4u tr_frag(unsigned tb, int off, int s, int ct) {
    v2u l2 = tr_rd(tb ^ (unsigned)(64 * ct), off + s * 4096), h2 = tr_rd(tb ^ (unsigned)(64 * ct + 16), off + s * 4096 + 1024);
    return v4u{l2[0], l2[1], h2[0], h2[1]};
}
DI void tr_frag_g(unsigned tb, int off, int g, v4u (&fr)[2][4]) { fr[g & 1][g >> 1] = tr_frag(tb, off, g & 1, g >> 1); }
DI void tr_chunk(unsigned tb, int off, v4u (&fr)[2][4]) {
#pragma unroll
    for (int g = 0; g < 8; ++g) tr_frag_g(tb, off, g, fr);
}
DI v4u tr2(unsigned a0, unsigned a1, int off) { v2u l2 = tr_rd(a0, off), h2 = tr_rd(a1, off + 1024); return v4u{l2[0], l2[1], h2[0], h2[1]}; }
DI void tro_frag_g(unsigned tb, int off, int g, v4u (&fr)[2][4]) {
    const unsigned a = tb ^ (unsigned)(32 * (g >> 1));
    v2u l2 = tr_rd(a, off + (g & 1) * 4096), h2 = tr_rd(a, off + (g & 1) * 4096 + 1024);
    fr[g & 1][g >> 1] = v4u{l2[0], l2[1], h2[0], h2[1]};
}
template <bool NEXT> DI void gemm_step(f32x16 (&acc)[4], const v4u (&cur)[2][4], v4u b0, v4u b1, unsigned tb, int off, v4u (&nxt)[2][4]) {
#pragma unroll
    for (int g = 0; g < 8; ++g) {
        acc[g >> 1] = mfma32(cur[g & 1][g >> 1], (g & 1) ? b1 : b0, acc[g >> 1]);
        if (NEXT) tro_frag_g(tb, off, g, nxt);
        SB();
    }
}

__global__ __launch_bounds__(320, 2) void fused_kernel(const void* __restrict__ x, const void* __restrict__ K, const void* __restrict__ V,
                                                      const void* __restrict__ wq, const void* __restrict__ wo, void* __restrict__ out, char* ws) {
    extern __shared__ __attribute__((aligned(16))) char smem[];
    LAS char* L = (LAS char*)smem;
    const unsigned Lb = (unsigned)(size_t)L;
    const int tid = threadIdx.x, lane = tid & 63, w = rfl(tid >> 6);
    const int xcd = blockIdx.x & 7, idx = blockIdx.x >> 3;
    const int hl = idx & 3, b = idx >> 2, h = xcd * 4 + hl, bh = b * 32 + h;
    const int wgid = bh;
    const __amdgpu_buffer_rsrc_t rws = mkrsrc(ws, WS_BYTES);
    const int cb0 = ((b >> 1) + hl) & 3, rotp = b % 3;
    u64 tag = 0;
    const int pos = lane & 15, rq = lane >> 4;
    auto wq_off = [&](int n) -> int { return ((((n >> 2) + cb0) & 3) * 128 + ((n & 3) ^ (2 * (b & 1))) * 32) * 8192 + h * 256; };
    {
        unsigned long long pc; int len;
        asm volatile("s_getpc_b64 %0\n.Lfk_begin:\n\ts_mov_b32 %1, .Lfk_end-.Lfk_begin" : "=s"(pc), "=s"(len));
        __builtin_amdgcn_raw_ptr_buffer_load_lds(mkrsrc((const void*)pc, len), (LAS void*)(L + CTL + 256), 4, lane * 4, (idx * 5 + w) * 256, 0, 0);
    }
    {
        const bool isx = w < 3;
        const int sh = isx ? 10 : 13;
        const __amdgpu_buffer_rsrc_t r0 = mkrsrc(isx ? (const char*)x + (size_t)b * 98304 : (const char*)wq, isx ? 98304 : 512 * 8192);
        int vo[4];
#pragma unroll
        for (int k = 0; k < 4; ++k) vo[k] = ((4 * k + rq) << sh) + 16 * (pos ^ (rq << 2) ^ k) - 1024 * k;
        dma_chunk<0>(r0, L + (isx ? X_BASE + w * SLOT : P_BASE + (w - 3) * SLOT), vo, isx ? w * 32768 + cb0 * 256 : wq_off(w - 3), 16 << sh);
    }

    if (w >= 3) {
        int vo1k[4];
#pragma unroll
        for (int k = 0; k < 4; ++k) vo1k[k] = (4 * k + rq) * 1024 + 16 * (pos ^ (rq << 2) ^ k) - 1024 * k;
        const __amdgpu_buffer_rsrc_t rwo = mkrsrc(wo, 4096 * 1024);
        auto wo_off = [&](int ci) -> int {
            int head = hl;
            if (ci >= 4) { int ps = (ci >> 2) - 1 + rotp; ps = ps >= 3 ? ps - 3 : ps; head = (hl + 1 + ps) & 3; }
            return ((xcd * 4 + head) * 128 + (ci & 3) * 32) * 1024 + hl * 128;
        };
        __builtin_amdgcn_s_setprio(3);
        unsigned prog = 0;
        unsigned fulla = Lb + CTL + (w == 3 ? C_FULLK : C_FULLW);
        OPAQUE(fulla);
        auto gate = [&](unsigned need) {
            if (__builtin_expect(prog < need, 0)) {
                for (unsigned sp = 0; prog < need && sp < SPIN_CAP; ++sp) { if (sp) __builtin_amdgcn_s_sleep(1); prog = lds_min3(Lb + CTL + C_G); }
            }
        };
        if (w == 3) {
            const __amdgpu_buffer_rsrc_t rx = mkrsrc((const char*)x + (size_t)b * 98304, 98304);
            const __amdgpu_buffer_rsrc_t rk = mkrsrc((const char*)K + (size_t)bh * 131072, 131072);
            const __amdgpu_buffer_rsrc_t rv = mkrsrc((const char*)V + (size_t)bh * 131072, 131072);
            int vo256[4];
#pragma unroll
            for (int k = 0; k < 4; ++k) vo256[k] = rq * 256 + 16 * (pos ^ (rq << 2) ^ k);
            const int trot = idx;
            tag = __hip_atomic_load((GAS u64*)(ws + WS_EPOCH + b * 256), RLX_AGENT) + 1;
#pragma unroll
            for (int i = 3; i < 12; ++i) {
                const int cbi = i / 3, r = i % 3;
                if (i >= 3 + X_D) { VMCNT(8 * (X_D - 1)); lds_st(fulla, i - X_D + 1); }
                if (i == 9) gate(1);
                dma_chunk<0>(rx, L + (cbi == 1 ? S_BASE + 2 * SLOT : (cbi == 2 ? S_BASE + 5 * SLOT : X_BASE)) + r * SLOT, vo1k, r * 32768 + ((cbi + cb0) & 3) * 256, 16384);
                if (i == 3 + X_D - 1) { VMCNT(8 * X_D); __builtin_amdgcn_s_barrier(); OPAQUE(tag); }
            }
            constexpr unsigned pubd = 12 - X_D;
#pragma unroll 1
            for (int c = 0; c < 32; ++c) {
                const bool isk = c < 5 || (c < 27 && !(c & 1));
                const int tile = c < 5 ? c : (c >= 27 ? c - 16 : (isk ? (c + 4) >> 1 : (c - 5) >> 1));
                LAS char* sl = L + ((c & 8) ? S_BASE : P_BASE) + (c & 7) * SLOT;
                VMCNT(8 * (KV_D - 1)); lds_st(fulla, max(pubd, (unsigned)(12 + c - KV_D + 1)));
                gate(c + 1);
                dma_chunk<KV_AUX>(isk ? rk : rv, sl, vo256, ((tile + trot) & 15) * 8192, 4096);
            }
#pragma unroll
            for (int d = KV_D - 1; d >= 0; --d) { VMCNT(8 * d); lds_st(fulla, 12 + 32 - d); }
            {
                int ps = (lane & 3) + rotp; ps = ps >= 3 ? ps - 3 : ps;
                const int pw = b * 32 + xcd * 4 + ((hl + 1 + ps) & 3);
                GAS u64* fp = (GAS u64*)(ws + WS_F1 + (pw * 3 + min((lane >> 2) & 3, 2)) * 128);
                unsigned m = 0;
                for (unsigned sp = 0; m != 0x777u; ++sp) {
                    const u64 f = __hip_atomic_load(fp, RLX_AGENT);
                    unsigned r = (unsigned)__builtin_amdgcn_ballot_w64(f == tag) & 0x777u;
                    if (sp >= SPIN_CAP) r = 0x777u;
                    if (r != m) { m = r; lds_st(Lb + CTL + C_RDY, m); }
                }
            }
        } else {
            const __amdgpu_buffer_rsrc_t rwq = mkrsrc(wq, 512 * 8192);
            int vo8k[4];
#pragma unroll
            for (int k = 0; k < 4; ++k) vo8k[k] = (4 * k + rq) * 8192 + 16 * (pos ^ (rq << 2) ^ k) - 1024 * k;
#pragma unroll
            for (int n = 2; n < 16; ++n) {
                if (n >= 2 + W_D) { VMCNT(8 * (W_D - 1)); lds_st(fulla, n - W_D + 1); }
                if (n == 10) gate(XS1 + 2);
                if (n == 13) gate(XS2 + 2);
                dma_chunk<0>(rwq, L + (n & 8 ? S_BASE : P_BASE) + (n & 7) * SLOT, vo8k, wq_off(n), 131072);
                if (n == 3) { VMCNT(16); __builtin_amdgcn_s_barrier(); }
            }
#pragma unroll
            for (int d = W_D - 1; d >= 0; --d) { VMCNT(8 * d); lds_st(fulla, 16 - d); }
            int vow[4];
#pragma unroll
            for (int k = 0; k < 4; ++k) vow[k] = (4 * k + rq) * 1024 + 16 * ((pos ^ (rq << 1)) & 7) + 512 * (pos >> 3) - 1024 * k;
#pragma unroll 1
            for (int i = 0; i < 16; ++i) {
                const int ci = i < 3 ? 8 + i : (i < 11 ? i - 3 : i);
                if (i == WO_HOLD_I) { for (unsigned sp = 0; rfl(lds_ld(Lb + CTL + C_FULLK)) < WO_HOLD_K && sp < SPIN_CAP; ++sp) __builtin_amdgcn_s_sleep(2); }
                gate(i < 3 ? WO_START : 30 + i);
                dma_chunk<0>(rwo, L + ci * SLOT, vow, wo_off(ci), 16384);
                VMCNT(8 * (WO_D - 1)); if (i + 2 > WO_D) lds_st(fulla, 16 + i + 2 - WO_D);
            }
#pragma unroll
            for (int d = WO_D - 2; d >= 0; --d) { VMCNT(8 * d); lds_st(fulla, 32 - d); }
        }
    } else {
        const int t = w;
        if (tid < 64) lds_st(Lb + CTL + tid * 4, 0u);
        tag = __hip_atomic_load((GAS u64*)(ws + WS_EPOCH + b * 256), RLX_AGENT) + 1;
        const int r32 = lane & 31, hh = lane >> 5;
        volatile LAS unsigned* ctl = (volatile LAS unsigned*)(L + CTL);
        const int fr_ = ((r32 & 3) << 2) | ((r32 >> 2) & 3);
        unsigned rbx = Lb + t * SLOT + ((256 * r32 + 16 * (hh ^ fr_)) ^ (128 * (b & 1)));
        const int pr = (r32 & 19) | ((r32 & 4) << 1) | ((r32 & 8) >> 1);
        const int fp_ = ((pr & 3) << 2) | ((pr >> 2) & 3);
        unsigned rbk = Lb + P_BASE + 256 * pr + 16 * (hh ^ fp_);
        const int blk = (lane >> 4) & 1, q4 = (lane & 15) >> 2, p4 = lane & 3;
        unsigned tb0 = Lb + 256 * (8 * hh + q4) + 64 * q4 + 16 * ((2 * blk + (p4 & 1)) ^ (2 * hh)) + 8 * (p4 >> 1);
        unsigned availW = 2, availK = 0;
        auto waitW = [&](unsigned need) { if (__builtin_expect(availW < need, 0)) for (unsigned sp = 0; availW < need && sp < SPIN_CAP; ++sp) { if (sp) __builtin_amdgcn_s_sleep(1); availW = rfl(ctl[C_FULLW / 4]); } CBAR(); };
        auto waitKs = [&](unsigned need) { if (__builtin_expect(availK < need, 0)) for (unsigned sp = 0; availK < need && sp < SPIN_CAP; ++sp) { if (sp) __builtin_amdgcn_s_sleep(1); availK = rfl(ctl[C_FULLK / 4]); } CBAR(); };
        auto waitK = [&](unsigned n) { waitKs(12 + n); };
        f32x16 acc[4];
#pragma unroll
        for (int d = 0; d < 4; ++d) acc[d] = f32x16{};
        {
            v4u fa[2][4], fb[2][4], xf[32];
            const unsigned tbw0 = tb0 + P_BASE, tbw1 = tb0 + S_BASE;
            asm volatile("s_waitcnt vmcnt(0) lgkmcnt(0)" ::: "memory");
            __builtin_amdgcn_s_barrier();
            CBAR();
            xf[0] = row_rd(rbx + X_BASE); xf[1] = row_rd((rbx + X_BASE) ^ 32u);
            tr_chunk(tbw0, 0, fa);
#pragma unroll
            for (int ks = 2; ks < 8; ++ks) xf[ks] = row_rd(((rbx + X_BASE) ^ (unsigned)(32 * ks)));
            ctl[C_G / 4 + t] = 1;
#pragma unroll
            for (int n = 0; n < 16; ++n) {
                const int xq = n == XS1 ? 1 : (n == XS2 ? 2 : (n == XS3 ? 3 : 0));
                if (n & 1) waitW(n + 3 < 16 ? n + 3 : 16);
                if (xq) waitKs(3 * xq + t + 1);
                const int offn = ((n + 1) & 7) * SLOT;
                const unsigned tbw = n + 1 < 8 ? tbw0 : tbw1;
                const unsigned xn = rbx + (xq == 1 ? S_BASE + 2 * SLOT : (xq == 2 ? S_BASE + 5 * SLOT : X_BASE));
                v2u fw;
                SB();
#pragma unroll
                for (int g = 0; g < 8; ++g) {
                    if (n & 1) { acc[g >> 1] = mfma32(fb[g & 1][g >> 1], xf[2 * n + (g & 1)], acc[g >> 1]); if (n < 15) tr_frag_g(tbw, offn, g, fa); }
                    else { acc[g >> 1] = mfma32(fa[g & 1][g >> 1], xf[2 * n + (g & 1)], acc[g >> 1]); tr_frag_g(tbw, offn, g, fb); }
                    if (xq) xf[8 * xq + g] = row_rd(xn ^ (unsigned)(32 * g));
                    if (g == 3 && !(n & 1)) fw = *(volatile LAS v2u*)(L + CTL);
                    if (g == 0 && !(n & 1) && n > 0) ctl[C_G / 4 + t] = n + 1;
                    if (g == 7 && n == 14) ctl[C_G / 4 + t] = 16;
                    if (g == 7 && !(n & 1)) { availK = rfl(fw[0]); availW = max(2u, rfl(fw[1])); }
                    SB();
                }
            }
        }
        asm volatile("" :: "v"(tag));
        constexpr float CS = 0.08838834764831845f * 1.4426950408889634f;
        constexpr float THR = 8.0f;
        v4u qf[8], kf[8];
        unsigned ka[8], va[8];
#pragma unroll
        for (int i = 0; i < 8; ++i) { ka[i] = rbk ^ (unsigned)(32 * i); va[i] = (tb0 + P_BASE) ^ (unsigned)(64 * (i >> 1) + 16 * (i & 1)); OPAQUE(ka[i]); OPAQUE(va[i]); }
        waitK(2);
#pragma unroll
        for (int ks = 0; ks < 8; ++ks) kf[ks] = row_rd(ka[ks]);
#pragma unroll
        for (int d = 0; d < 4; ++d) { acc[d] *= CS; qf[2 * d] = pack8(acc[d], 0); qf[2 * d + 1] = pack8(acc[d], 1); }
#pragma unroll
        for (int d = 0; d < 4; ++d) acc[d] = f32x16{};
        float lsum = 0.f;
        f32x16 sA, sB, negm; v4u vf[8], pe[4][2], cw;
        unsigned ctla = Lb + CTL + C_FULLK, pka = Lb + CTL + C_G + 4 * t, relv = 17;
        OPAQUE(ctla); OPAQUE(pka); OPAQUE(relv);
        auto decide = [&](float tm, f32x16& nxt, int dead) {
            if (__builtin_expect(__builtin_amdgcn_ballot_w64(tm > THR) != 0ull, 0)) {
                const float dl = fmaxf(swapmax(tm), 0.f), f = __builtin_amdgcn_exp2f(-dl);
                lsum *= f;
#pragma unroll
                for (int d = 0; d < 4; ++d) acc[d] *= f;
#pragma unroll
                for (int i = 0; i < 16; ++i) { nxt[i] -= dl; negm[i] -= dl; }
#pragma unroll
                for (int q = 0; q < 4; ++q) if (!(dead >> q & 1)) {
#pragma unroll
                    for (int i = 0; i < 8; ++i) { const unsigned u = pe[q][i >> 2][i & 3]; pe[q][i >> 2][i & 3] = cvtpk(__uint_as_float(u << 16) * f, __uint_as_float(u & 0xffff0000u) * f); }
                }
            }
        };
        auto block = [&](f32x16& cur, f32x16& nxt, int pw, int pr, int vslot, int kslot, unsigned rinc) -> float {
            float m = 0.f, m1 = 0.f, s0 = lsum, s1 = 0.f; unsigned fk = 0;
            SB();
#pragma unroll
            for (int g = 0; g < 16; ++g) {
                if (g < 8) nxt = mfma32(kf[g], qf[g], g == 0 ? negm : nxt);
                else acc[(g - 8) >> 1] = mfma32(vf[g - 8], pe[pr][g & 1], acc[(g - 8) >> 1]);
                if (g >= 3 && g < 11) { const int i = g - 3; vf[i] = tr2(va[2 * (i >> 1)], va[2 * (i >> 1) + 1], vslot * SLOT + (i & 1) * 4096); }
                if (g >= 11 && g < 15) { const int i = 2 * (g - 11); kf[i] = row_rd(ka[i] + kslot * SLOT); kf[i + 1] = row_rd(ka[i + 1] + kslot * SLOT); }
                const int e0 = g < 3 ? 2 * g : g + 3, e1 = g < 3 ? 2 * g + 2 : g + 4, p0 = g == 0 ? 0 : (g - 1 < 3 ? 2 * (g - 1) : g + 2);
#pragma unroll
                for (int e = e0; e < e1 && e < 16; ++e) cur[e] = __builtin_amdgcn_exp2f(cur[e]);
#pragma unroll
                for (int e = p0; e < e0 && e < 16; ++e) { if (e & 1) { s1 = fadd_s(s1, cur[e]); pe[pw][e >> 3][(e >> 1) & 3] = cvtpk(cur[e - 1], cur[e]); } else s0 = fadd_s(s0, cur[e]); }
                if (g == 11) { m = max3f(nxt[0], nxt[1], nxt[2]); m1 = max3f(nxt[3], nxt[4], nxt[5]); }
                if (g == 12) { m = max3f(m, nxt[6], nxt[7]); m1 = max3f(m1, nxt[8], nxt[9]); }
                if (g == 13) { m = max3f(m, nxt[10], nxt[11]); m1 = max3f(m1, nxt[12], nxt[13]); }
                if (g == 14) { m = max3f(m, nxt[14], nxt[15]); m = max3f(m, m1, m1); relv += rinc; *(volatile LAS unsigned*)(size_t)pka = relv; }
                if (g == 10) fk = *(volatile LAS unsigned*)(size_t)ctla;
                if (g == 15) { availK = rfl(fk); lsum = s0 + s1; OPAQUE(lsum); }
                SB();
            }
            return m;
        };
        auto estep = [&](f32x16& cur, f32x16& nxt, int pw, int kslot) -> float {
            float s0 = 0.f, s1 = 0.f;
            SB();
#pragma unroll
            for (int g = 0; g < 8; ++g) {
                nxt = mfma32(kf[g], qf[g], g == 0 ? negm : nxt);
                kf[g] = row_rd(ka[g] + kslot * SLOT);
                cur[2 * g] = __builtin_amdgcn_exp2f(cur[2 * g]); cur[2 * g + 1] = __builtin_amdgcn_exp2f(cur[2 * g + 1]);
                if (g > 0) { pe[pw][(g - 1) >> 2][(g - 1) & 3] = cvtpk(cur[2 * g - 2], cur[2 * g - 1]); s0 = fadd_s(s0, cur[2 * g - 2]); s1 = fadd_s(s1, cur[2 * g - 1]); }
                SB();
            }
            relv += 1; *(volatile LAS unsigned*)(size_t)pka = relv;
            pe[pw][1][3] = cvtpk(cur[14], cur[15]);
            lsum += (s0 + cur[14]) + (s1 + cur[15]);
            float m = fmaxf(nxt[0], nxt[1]);
#pragma unroll
            for (int i = 2; i < 16; i += 2) m = fmaxf(fmaxf(m, nxt[i]), nxt[i + 1]);
            return m;
        };
#pragma unroll
        for (int ks = 0; ks < 8; ++ks) { sA = mfma32(kf[ks], qf[ks], ks == 0 ? f32x16{} : sA); kf[ks] = row_rd(ka[ks] + SLOT); SB(); }
        relv += 1; *(volatile LAS unsigned*)(size_t)pka = relv;
        {
            float m = fmaxf(sA[0], sA[1]);
#pragma unroll
            for (int i = 2; i < 16; i += 2) m = fmaxf(fmaxf(m, sA[i]), sA[i + 1]);
            m = swapmax(m);
#pragma unroll
            for (int i = 0; i < 16; ++i) { sA[i] -= m; negm[i] = -m; }
            OPAQUE(negm);
        }
        waitK(5);
        decide(estep(sA, sB, 0, 2), sB, 14);
        decide(estep(sB, sA, 1, 3), sA, 12);
        decide(estep(sA, sB, 2, 4), sB, 8);
#pragma unroll 1
        for (int j = 0; j < 12; j += 4) {
            waitK(2 * j + 7); decide(block(sB, sA, 3, 0, 5, 6, 2), sA, 1);
#pragma unroll
            for (int i = 0; i < 8; ++i) { ka[i] ^= S_BASE; OPAQUE(ka[i]); }
            waitK(2 * j + 9); decide(block(sA, sB, 0, 1, 7, 0, 2), sB, 2);
#pragma unroll
            for (int i = 0; i < 8; ++i) { va[i] ^= S_BASE; OPAQUE(va[i]); }
            waitK(2 * j + 11); decide(block(sB, sA, 1, 2, 1, 2, 2), sA, 4);
            waitK(j == 8 ? 28 : 2 * j + 13); decide(block(sA, sB, 2, 3, 3, 4, j == 8 ? 1 : 2), sB, 8);
        }
        const int l16 = lane * 16;
        v4u pf[3][8];
        unsigned got = 0;
        auto rdy = [&]() -> unsigned { const unsigned r = (rfl(ctl[C_RDY / 4]) >> (4 * t)) & 7u & ~got; CBAR(); return r; };
        auto take = [&](int k) {
            got |= 1u << k;
            int ps = k + rotp; ps = ps >= 3 ? ps - 3 : ps;
            const int so = (int)WS_O + (b * 32 + xcd * 4 + ((hl + 1 + ps) & 3)) * 24576 + t * 8192;
#pragma unroll
            for (int i = 0; i < 8; ++i) pf[k][i] = __builtin_amdgcn_raw_buffer_load_b128(rws, l16 + (i & 3) * 1024, so + (i >> 2) * 4096, 16);
        };
        auto must = [&](int k) {
            unsigned r = rdy();
            if (!(got >> k & 1)) { for (unsigned sp = 0; !(r >> k & 1) && sp < 4 * SPIN_CAP; ++sp) r = rdy(); take(k); }
#pragma unroll
            for (int j = k + 1; j < 3; ++j) if (r >> j & 1) take(j);
        };
        float inv = 0.f, s0 = lsum, s1 = 0.f;
        unsigned tbo = Lb + 256 * (8 * hh + q4) + 16 * ((8 * (p4 & 1) + blk) ^ (q4 << 1)) + 8 * (p4 >> 1);
        OPAQUE(tbo);
        v4u fa[2][4], fb[2][4];
        waitK(29);
        auto vfrag = [&](int slot, int g, v4u (&fr)[2][4]) { fr[g & 1][g >> 1] = tr2(va[2 * (g >> 1)], va[2 * (g >> 1) + 1], slot * SLOT + (g & 1) * 4096); };
#pragma unroll
        for (int g = 0; g < 8; ++g) vfrag(4, g, fa);
        cw = *(volatile LAS v4u*)(size_t)ctla;
        relv += 1; *(volatile LAS unsigned*)(size_t)pka = relv;
#pragma unroll
        for (int i = 0; i < 8; ++i) sB[i] = __builtin_amdgcn_exp2f(sB[i]);
        availK = rfl(cw[0]); availW = rfl(cw[1]);
#pragma unroll
        for (int e = 0; e < 4; ++e) {
            if (e < 3) waitK(30 + e); else waitW(20);
            SB();
#pragma unroll
            for (int g = 0; g < 8; ++g) {
                if (e & 1) { acc[g >> 1] = mfma32(fb[g & 1][g >> 1], pe[e][g & 1], acc[g >> 1]); if (e < 3) vfrag(5 + e, g, fa); else tro_frag_g(tbo, 0, g, fa); }
                else { acc[g >> 1] = mfma32(fa[g & 1][g >> 1], pe[e][g & 1], acc[g >> 1]); vfrag(5 + e, g, fb); }
                if (e == 0) sB[8 + g] = __builtin_amdgcn_exp2f(sB[8 + g]);
                if (e == 1) { pe[3][g >> 2][g & 3] = cvtpk(sB[2 * g], sB[2 * g + 1]); s0 = fadd_s(s0, sB[2 * g]); s1 = fadd_s(s1, sB[2 * g + 1]); }
                if (e == 2 && g == 1) { inv = __builtin_amdgcn_rcpf(swapsum(s0 + s1)); OPAQUE(inv); }
                if (g == 3) cw = *(volatile LAS v4u*)(size_t)ctla;
                if (g == 7) { availK = rfl(cw[0]); availW = rfl(cw[1]); }
                SB();
            }
        }
        v4u cur[8];
        {
            const __amdgpu_buffer_rsrc_t ro = mkrsrc(ws + WS_O + wgid * 24576 + t * 8192, 8192);
#pragma unroll
            for (int d = 0; d < 4; ++d) {
                acc[d] *= inv; cur[2 * d] = pack8(acc[d], 0); cur[2 * d + 1] = pack8(acc[d], 1);
                __builtin_amdgcn_raw_buffer_store_b128(cur[2 * d], ro, l16 + (2 * d) * 1024, 0, 16);
                __builtin_amdgcn_raw_buffer_store_b128(cur[2 * d + 1], ro, l16 + (2 * d + 1) * 1024, 0, 16);
            }
        }
#pragma unroll
        for (int d = 0; d < 4; ++d) acc[d] = f32x16{};
        auto gemm4 = [&](unsigned tb, const v4u* cur, auto FIRST, auto LAST) {
            OPAQUE(tb);
            SB();
            gemm_step<true>(acc, fa, cur[0], cur[1], tb, SLOT, fb);
            gemm_step<true>(acc, fb, cur[2], cur[3], tb, 2 * SLOT, fa);
            if (FIRST) {
                VMCNT(0);
                if (lane == 0) __hip_atomic_store((GAS u64*)(ws + WS_F1 + (wgid * 3 + t) * 128), tag, RLX_AGENT);
                { const unsigned r = rdy(); if (r & 1) take(0); if (r & 2) take(1); if (r & 4) take(2); }
                SB();
            }
            gemm_step<true>(acc, fa, cur[4], cur[5], tb, 3 * SLOT, fb);
            if (!LAST) gemm_step<true>(acc, fb, cur[6], cur[7], tb, 4 * SLOT, fa);
            else gemm_step<false>(acc, fb, cur[6], cur[7], 0, 0, fa);
        };
        {
            constexpr std::integral_constant<bool, true> T{};
            constexpr std::integral_constant<bool, false> F{};
            waitW(24);
            gemm4(tbo, cur, T, F);
            must(0); waitW(27);
            gemm4(tbo + 4 * SLOT, pf[0], F, F);
            must(1); waitW(29);
            gemm4(tbo + 8 * SLOT, pf[1], F, T);
            must(2); waitW(32);
            const __amdgpu_buffer_rsrc_t rp = mkrsrc(ws + WS_P + wgid * 24576 + t * 8192, 8192);
            constexpr int KS = 8, M = 32;
            unsigned tb = tbo + 12 * SLOT;
            OPAQUE(tb);
            v4u fr[8], st0, st1;
            auto rd = [&](int i) { const int ct = i / KS, ks = i % KS; const unsigned ad = tb ^ (unsigned)(32 * ct); fr[i & 7] = tr2(ad, ad, ks * 4096); };
#pragma unroll
            for (int i = 0; i < 8; ++i) rd(i);
            SB();
#pragma unroll
            for (int i = 0; i < M; ++i) {
                const int ct = i / KS, ks = i % KS;
                acc[ct] = mfma32(fr[i & 7], pf[2][ks], acc[ct]);
                if (i + 8 < M) rd(i + 8);
                if (ct > 0) {
                    if (ks == 2) st0 = pack8(acc[ct - 1], 0);
                    if (ks == 3) { st1 = pack8(acc[ct - 1], 1); __builtin_amdgcn_raw_buffer_store_b128(st0, rp, l16 + (2 * ct - 2) * 1024, 0, 16); }
                    if (ks == 4) __builtin_amdgcn_raw_buffer_store_b128(st1, rp, l16 + (2 * ct - 1) * 1024, 0, 16);
                }
                SB();
            }
            __builtin_amdgcn_raw_buffer_store_b128(pack8(acc[3], 0), rp, l16 + 6 * 1024, 0, 16);
            __builtin_amdgcn_raw_buffer_store_b128(pack8(acc[3], 1), rp, l16 + 7 * 1024, 0, 16);
        }
        ctl[C_DONE / 4] = 1;
        VMCNT(0);
        if (lane < 2) __hip_atomic_store((GAS u64*)(ws + WS_F2 + ((wgid * 3 + t) * 2 + lane) * 128), tag, RLX_AGENT);
    }
    if (xcd < 6 && w < 4) {
        const int t = xcd >> 1, f = 4 * (xcd & 1) + w;
        if (w == 3) {
            for (unsigned sp = 0; rfl(lds_ld(Lb + CTL + C_DONE)) == 0u && sp < SPIN_CAP; ++sp) __builtin_amdgcn_s_sleep(2);
            GAS u64* fp = (GAS u64*)(ws + WS_F2 + (((b * 32 + (lane & 7) * 4 + hl) * 3 + t) * 2 + (xcd & 1)) * 128);
            for (unsigned sp = 0; sp < SPIN_CAP; ++sp) {
                const u64 fl = __hip_atomic_load(fp, RLX_AGENT);
                if (__builtin_amdgcn_ballot_w64(fl != tag) == 0ull) break;
            }
            lds_st(Lb + CTL + C_READY, 1);
            if (xcd == 0 && hl == 0 && lane == 0) __hip_atomic_store((GAS u64*)(ws + WS_EPOCH + b * 256), tag, RLX_AGENT);
        } else {
            for (unsigned sp = 0; rfl(lds_ld(Lb + CTL + C_READY)) == 0u && sp < SPIN_CAP; ++sp) {}
        }
        CBAR();
        v4u pp[8];
#pragma unroll
        for (int xx = 0; xx < 8; ++xx) pp[xx] = __builtin_amdgcn_raw_buffer_load_b128(rws, lane * 16, (int)WS_P + (b * 32 + xx * 4 + hl) * 24576 + t * 8192 + f * 1024, 16);
        float s[8];
#pragma unroll
        for (int k = 0; k < 8; ++k) s[k] = 0.f;
#pragma unroll
        for (int xx = 0; xx < 8; ++xx)
#pragma unroll
            for (int k = 0; k < 4; ++k) { s[2 * k] += __uint_as_float(pp[xx][k] << 16); s[2 * k + 1] += __uint_as_float(pp[xx][k] & 0xffff0000u); }
        v4u o = {cvtpk(s[0], s[1]), cvtpk(s[2], s[3]), cvtpk(s[4], s[5]), cvtpk(s[6], s[7])};
        *(v4u*)((char*)out + ((size_t)(b * 96 + 32 * t + (lane & 31)) * 512 + 64 * hl + 256 * (lane >> 5) + 8 * f) * 2) = o;
    }
}

__device__ __attribute__((used, noinline)) void fused_kernel_end_marker() { asm volatile(".Lfk_end:"); }

extern "C" void kernel_launch(void* const* d_in, const int* in_sizes, int n_in, void* d_out, int out_size, void* d_ws, size_t ws_size, hipStream_t stream) {
    (void)hipFuncSetAttribute((const void*)fused_kernel, hipFuncAttributeMaxDynamicSharedMemorySize, LDS_BYTES);
    fused_kernel<<<256, 320, LDS_BYTES, stream>>>(d_in[0], d_in[1], d_in[2], d_in[3], d_in[4], d_out, (char*)d_ws);
}
```
